```python
import torch

def _softmax(x, axis=-1):
    return torch.softmax(x, dim=axis)

def _gelu(x):
    return torch.nn.functional.gelu(x, approximate='tanh')

def _silu(x):
    return torch.nn.functional.silu(x)

def _sigmoid(x):
    return torch.sigmoid(x)

def _layernorm(x, axis=-1, eps=1e-05):
    m = x.mean(dim=axis, keepdim=True)
    v = x.var(dim=axis, keepdim=True, unbiased=False)
    return (x - m) / torch.sqrt(v + eps)

def _scatter_add(tgt, idx, src, axis=0):
    return tgt.clone().index_add_(axis, idx, src)

def _segment_sum(src, seg, n):
    out = torch.zeros((n,) + src.shape[1:], dtype=src.dtype)
    return out.index_add_(0, seg, src)

def _pad(x, pw):
    flat = tuple((v for pair in reversed(pw) for v in pair))
    return torch.nn.functional.pad(x, flat)
kernel_name = 'sequential_preLN__memory_aug_recurrent_mem_tr__dense_activation_mish__prompt'

def setup_inputs():

    _w = lambda a: (lambda t: t.to(torch.bfloat16) if t.is_floating_point() else t)(torch.as_tensor(a))

    def _A_setup():
        B, S, Dm = (1, 2048, 640)
        H, Dh = (32, 192)
        M, seg = (8, 32)
        seg = min(seg, S)
        S = S // seg * seg
        return {'x': _w((lambda *s: torch.randn(*s, dtype=torch.bfloat16) * 0.05)(B, S, Dm)), 'mem0': _w((lambda *s: torch.randn(*s, dtype=torch.bfloat16) * 0.2)(B, M, Dm)), 'Wq': _w((lambda *s: torch.randn(*s, dtype=torch.bfloat16) * 1.6)(Dm, H, Dh) * Dm ** (-0.5)), 'Wk': _w((lambda *s: torch.randn(*s, dtype=torch.bfloat16) * 1.6)(Dm, H, Dh) * Dm ** (-0.5)), 'Wv': _w((lambda *s: torch.randn(*s, dtype=torch.bfloat16) * 0.2)(Dm, H, Dh) * Dm ** (-0.5)), 'Wo': _w((lambda *s: torch.randn(*s, dtype=torch.bfloat16) * 0.2)(H, Dh, Dm) * (H * Dh) ** (-0.5)), 'seg_len': seg}
    _A_in = _A_setup()

    def _B_setup():
        B, S, D, F = (1, 2048, 640, 1536)
        return {'x': _w((lambda *s: torch.randn(*s, dtype=torch.bfloat16) * 0.05)(B, S, D)), 'w1': _w((lambda *s: torch.randn(*s, dtype=torch.bfloat16) * 0.8)(D, F) * D ** (-0.5)), 'b1': _w((lambda *s: torch.randn(*s, dtype=torch.bfloat16) * 0.2)(F)), 'w2': _w((lambda *s: torch.randn(*s, dtype=torch.bfloat16) * 0.1)(F, D) * F ** (-0.5))}
    _B_in = _B_setup()
    return {'A_x': _A_in['x'], 'A_mem0': _A_in['mem0'], 'A_Wq': _A_in['Wq'], 'A_Wk': _A_in['Wk'], 'A_Wv': _A_in['Wv'], 'A_Wo': _A_in['Wo'], 'A_seg_len': _A_in['seg_len'], 'B_x': _B_in['x'], 'B_w1': _B_in['w1'], 'B_b1': _B_in['b1'], 'B_w2': _B_in['w2']}

def reference(A_x, A_mem0, A_Wq, A_Wk, A_Wv, A_Wo, A_seg_len, B_x, B_w1, B_b1, B_w2):

    def _A_ref(x, mem0, Wq, Wk, Wv, Wo, seg_len):
        B, S, Dm = x.shape
        H, Dh = (Wq.shape[1], Wq.shape[2])
        M = mem0.shape[1]
        scale = Dh ** (-0.5)
        n_seg = S // seg_len
        L = M + seg_len
        idx = torch.arange(L)
        mask = (idx[:, None] < M) | (idx[:, None] >= idx[None, :])
        mem = mem0
        outs = []
        for s in range(n_seg):
            xs = x[:, s * seg_len:(s + 1) * seg_len, :].clone()
            h = torch.concatenate([mem, xs], axis=1)
            q = torch.einsum('btd,dhe->bhte', h, Wq)
            k = torch.einsum('btd,dhe->bhte', h, Wk)
            v = torch.einsum('btd,dhe->bhte', h, Wv)
            scores = torch.einsum('bhqe,bhke->bhqk', q, k) * scale
            scores = torch.where(mask[None, None], scores, -1000000000.0)
            p = _softmax(scores, axis=-1)
            o = torch.einsum('bhqk,bhke->bhqe', p, v)
            o = torch.einsum('bhte,hed->btd', o, Wo)
            mem = o[:, :M, :].clone()
            outs.append(o[:, M:, :])
        return torch.concatenate(outs, axis=1)

    def _B_ref(x, w1, b1, w2):
        h = torch.einsum('bsd,df->bsf', x, w1) + b1
        sp = torch.log1p(torch.exp(-torch.abs(h))) + torch.where(h > 0, h, torch.zeros_like(h))
        act = h * torch.tanh(sp)
        return torch.einsum('bsf,fd->bsd', act, w2)
    x = A_x
    h = x + _A_ref(_layernorm(x, axis=-1), A_mem0, A_Wq, A_Wk, A_Wv, A_Wo, A_seg_len)
    h2 = h + _B_ref(_layernorm(h, axis=-1), B_w1, B_b1, B_w2)
    return h2
```

```cpp
#include <hip/hip_runtime.h>
#include <hip/hip_bf16.h>
#include <stdint.h>

typedef __bf16 bf16x8 __attribute__((ext_vector_type(8)));
typedef float f32x4 __attribute__((ext_vector_type(4)));
typedef unsigned u32x4 __attribute__((ext_vector_type(4)));
typedef unsigned u32x2 __attribute__((ext_vector_type(2)));
typedef unsigned short u16;

constexpr size_t R_M = 0;
constexpr size_t R_P = 2097152;
constexpr size_t R_Z = 4194304;
constexpr size_t R_FLAG = 6291456;
constexpr size_t O_CNT = 6381568;
constexpr size_t O_ABORT = 6385664;
constexpr size_t O_FLIP = 6385792;
constexpr size_t O_M0 = 6389760;
constexpr size_t R_XN = 8388608;
constexpr size_t R_H = 29360128;
constexpr size_t R_ACT = 33554432;
constexpr size_t ZSLOT = 655360;
constexpr size_t O_TK = 1572864;
constexpr size_t O_REQE = 6422528;
constexpr size_t O_ACKE = 6426624;
constexpr size_t O_REQL = 6430720;
constexpr size_t O_ACKL = 6434816;
constexpr size_t O_CNT1 = 6438912;
constexpr size_t O_CNT2 = 6439040;
constexpr size_t O_FLIPS = 6439168;
constexpr size_t R_DBG = 41943040;
#define ROLE_CLOBBERS "v0", "v1", "v2", "v3", "v4", "v5", "v6", "v7", "v8", "v9", "v10", "v11", "v12", "v13", "v14", "v15", "v16", "v17", "v18", "v19", "v20", "v21", "v22", "v23", "v24", "v25", "v26", "v27", "v28", "v29", "v30", "v31", "v32", "v33", "v34", "v35", "v36", "v37", "v38", "v39", "v40", "v41", "v42", "v43", "v44", "v45", "v46", "v47", "v48", "v49", "v50", "v51", "v52", "v53", "v54", "v55", "v56", "v57", "v58", "v59", "v60", "v61", "v62", "v63", "v64", "v65", "v66", "v67", "v68", "v69", "v70", "v71", "v72", "v73", "v74", "v75", "v76", "v77", "v78", "v79", "v80", "v81", "v82", "v83", "v84", "v85", "v86", "v87", "v88", "v89", "v90", "v91", "v92", "v93", "v94", "v95", "v96", "v97", "v98", "v99", "v100", "v101", "v102", "v103", "v104", "v105", "v106", "v107", "v108", "v109", "v110", "v111", "v112", "v113", "v114", "v115", "v116", "v117", "v118", "v119", "v120", "v121", "v122", "v123", "v124", "v125", "v126", "v127", "v128", "v129", "v130", "v131", "v132", "v133", "v134", "v135", "v136", "v137", "v138", "v139", "v140", "v141", "v142", "v143", "v144", "v145", "v146", "v147", "v148", "v149", "v150", "v151", "v152", "v153", "v154", "v155", "v156", "v157", "v158", "v159", "v160", "v161", "v162", "v163", "v164", "v165", "v166", "v167", "v168", "v169", "v170", "v171", "v172", "v173", "v174", "v175", "v176", "v177", "v178", "v179", "v180", "v181", "v182", "v183", "v184", "v185", "v186", "v187", "v188", "v189", "v190", "v191", "v192", "v193", "v194", "v195", "v196", "v197", "v198", "v199", "v200", "v201", "v202", "v203", "v204", "v205", "v206", "v207", "v208", "v209", "v210", "v211", "v212", "v213", "v214", "v215", "v216", "v217", "v218", "v219", "v220", "v221", "v222", "v223", "v224", "v225", "v226", "v227", "v228", "v229", "v230", "v231", "v232", "v233", "v234", "v235", "v236", "v237", "v238", "v239", "v240", "v241", "v242", "v243", "v244", "v245", "v246", "v247", "v248", "v249", "v250", "v251", "v252", "v253", "v254", "v255", "a0", "a1", "a2", "a3", "a4", "a5", "a6", "a7", "a8", "a9", "a10", "a11", "a12", "a13", "a14", "a15", "a16", "a17", "a18", "a19", "a20", "a21", "a22", "a23", "a24", "a25", "a26", "a27", "a28", "a29", "a30", "a31", "a32", "a33", "a34", "a35", "a36", "a37", "a38", "a39", "a40", "a41", "a42", "a43", "a44", "a45", "a46", "a47", "a48", "a49", "a50", "a51", "a52", "a53", "a54", "a55", "a56", "a57", "a58", "a59", "a60", "a61", "a62", "a63", "a64", "a65", "a66", "a67", "a68", "a69", "a70", "a71", "a72", "a73", "a74", "a75", "a76", "a77", "a78", "a79", "a80", "a81", "a82", "a83", "a84", "a85", "a86", "a87", "a88", "a89", "a90", "a91", "a92", "a93", "a94", "a95", "a96", "a97", "a98", "a99", "a100", "a101", "a102", "a103", "a104", "a105", "a106", "a107", "a108", "a109", "a110", "a111", "a112", "a113", "a114", "a115", "a116", "a117", "a118", "a119", "a120", "a121", "a122", "a123", "a124", "a125", "a126", "a127", "a128", "a129", "a130", "a131", "a132", "a133", "a134", "a135", "a136", "a137", "a138", "a139", "a140", "a141", "a142", "a143", "a144", "a145", "a146", "a147", "a148", "a149", "a150", "a151", "a152", "a153", "a154", "a155", "a156", "a157", "a158", "a159", "a160", "a161", "a162", "a163", "a164", "a165", "a166", "a167", "a168", "a169", "a170", "a171", "a172", "a173", "a174", "a175", "a176", "a177", "a178", "a179", "a180", "a181", "a182", "a183", "a184", "a185", "a186", "a187", "a188", "a189", "a190", "a191", "a192", "a193", "a194", "a195", "a196", "a197", "a198", "a199", "a200", "a201", "a202", "a203", "a204", "a205", "a206", "a207", "a208", "a209", "a210", "a211", "a212", "a213", "a214", "a215", "a216", "a217", "a218", "a219", "a220", "a221", "a222", "a223", "a224", "a225", "a226", "a227", "a228", "a229", "a230", "a231", "a232", "a233", "a234", "a235", "a236", "a237", "a238", "a239", "a240", "a241", "a242", "a243", "a244", "a245", "a246", "a247", "a248", "a249", "a250", "a251", "a252", "a253", "a254", "a255", "s18", "s19", "s20", "s21", "s22", "s23", "s24", "s25", "s26", "s27", "s28", "s29", "s30", "s31", "s34", "s35", "s36", "s37", "s38", "s39", "s40", "s41", "s42", "s43", "s44", "s45", "s46", "s47", "s48", "s49", "s50", "s51", "s52", "s53", "s54", "s55", "s56", "s57", "s58", "s59", "s60", "s61", "s62", "s63", "s64", "s65", "s66", "s67", "s68", "s69", "s70", "s71", "s72", "s73", "s74", "s75", "s76", "s77", "s78", "s79", "s80", "s81", "s82", "s83", "s84", "s85", "s86", "s87", "s88", "s89", "s90", "s91", "s92", "s93", "s94", "s95", "s96", "s97", "s98", "s99", "vcc", "scc", "memory"

__device__ __forceinline__ float bf_lo(unsigned u) { return __uint_as_float(u << 16); }
__device__ __forceinline__ float bf_hi(unsigned u) { return __uint_as_float(u & 0xffff0000u); }
__device__ __forceinline__ unsigned pk_bf16(float a, float b) {
    typedef __bf16 bf16x2 __attribute__((ext_vector_type(2)));
    bf16x2 r = {(__bf16)a, (__bf16)b};
    return __builtin_bit_cast(unsigned, r);
}
constexpr int PROBES = 4;
constexpr unsigned WARM_N = 0;
constexpr int WARM_NAP = 6, WARM_DELAY = 0;

typedef __attribute__((address_space(1))) unsigned gu32;
typedef __attribute__((address_space(1))) unsigned long long gu64;
#define RLX_AGENT __ATOMIC_RELAXED, __HIP_MEMORY_SCOPE_AGENT
__device__ __forceinline__ unsigned ld_dev(const char* p) { return __hip_atomic_load((gu32*)p, RLX_AGENT); }
__device__ __forceinline__ void st_dev(char* p, unsigned x) { __hip_atomic_store((gu32*)p, x, RLX_AGENT); }
__device__ __forceinline__ void st16_dev(char* p, u32x4 x) { asm volatile("global_store_dwordx4 %0, %1, off sc1\n\ts_nop 1" :: "v"(p), "v"(x) : "memory"); }
__device__ __forceinline__ void drain() { asm volatile("s_waitcnt vmcnt(0)" ::: "memory"); }
constexpr unsigned BOOT_SPIN = 1u << 18;

__device__ __forceinline__ void ln_load(u32x4 (&d)[5], const u16* __restrict__ x, int tile, int tid) {
    const u32x4* row = (const u32x4*)(x + (size_t)(tile * 16 + (tid >> 4)) * 640);
#pragma unroll
    for (int i = 0; i < 5; ++i) d[i] = row[(tid & 15) + 16 * i];
}
__device__ __forceinline__ void ln_tile(const u32x4 (&d)[5], char* wb, int tile, int tid) {
    const int r = tid >> 4, c = tid & 15;
    float sum = 0.f;
#pragma unroll
    for (int i = 0; i < 5; ++i)
#pragma unroll
        for (int k = 0; k < 4; ++k) sum += bf_lo(d[i][k]) + bf_hi(d[i][k]);
#pragma unroll
    for (int o = 1; o < 16; o <<= 1) sum += __shfl_xor(sum, o);
    const float mean = sum * (1.f / 640.f);
    float var = 0.f;
#pragma unroll
    for (int i = 0; i < 5; ++i)
#pragma unroll
        for (int k = 0; k < 4; ++k) { const float a = bf_lo(d[i][k]) - mean, e = bf_hi(d[i][k]) - mean; var += a * a + e * e; }
#pragma unroll
    for (int o = 1; o < 16; o <<= 1) var += __shfl_xor(var, o);
    const float rs = 1.f / sqrtf(var * (1.f / 640.f) + 1e-5f);
    char* dst = wb + R_XN + (size_t)(tile >> 1) * 40960 + (tile & 1) * 1024 + r * 16;
#pragma unroll
    for (int i = 0; i < 5; ++i) {
        const int ch = c + 16 * i;
        u32x4 o;
#pragma unroll
        for (int k = 0; k < 4; ++k) o[k] = pk_bf16((bf_lo(d[i][k]) - mean) * rs, (bf_hi(d[i][k]) - mean) * rs);
        st16_dev(dst + (ch >> 2) * 2048 + (ch & 3) * 256, o);
    }
}

__device__ __forceinline__ unsigned boot(const u16* __restrict__ x, const u16* __restrict__ mem0, const u16* Wq, const u16* Wk, const u16* Wv,
                                         const u16* Wo, char* wb, unsigned e, unsigned side, unsigned xq, unsigned rank, unsigned wave, char* smem) {
    const int tid = threadIdx.x;
    if (e == 124u) {
        if (wave == 0u) {
            unsigned p[2], q[8], dn = tid >= 16 && tid <= 60 ? 2u : 0u, done = 0, fl = 0;
            bool first = true, first2 = true;
            for (unsigned spins = 0; spins < BOOT_SPIN; ++spins) {
                unsigned r0[2], r[8];
#pragma unroll
                for (int i = 0; i < 2; ++i) r0[i] = ld_dev(wb + O_REQE + i * 2048 + tid * 32);
#pragma unroll
                for (int i = 0; i < 8; ++i) r[i] = ld_dev(wb + O_REQL + (i * 64 + tid) * 4);
                if (first2) {
#pragma unroll
                    for (int i = 0; i < 2; ++i) {
                        if (!first && ((r0[i] ^ p[i]) & 0xbfffffffu) == 0x80000000u) dn |= 1u << i;
                        else if (!(dn >> i & 1u) && (first || r0[i] != p[i])) { st_dev(wb + O_ACKE + i * 2048 + tid * 32, r0[i]); p[i] = r0[i]; }
                    }
                    first = false;
                    if (!__all(dn == 3u)) continue;
                    fl = (unsigned)(__ballot((r0[1] ^ p[1]) >> 30 & 1u) >> 61);
#pragma unroll
                    for (int i = 0; i < 2; ++i) __hip_atomic_store((gu64*)(wb + O_ACKE + i * 2048 + tid * 32), ((unsigned long long)fl << 32) | r0[i], RLX_AGENT);
                    p[1] = r0[1];
                    if (tid < 16 || tid >= 60) done = 256u;
                } else if (done < 256u) {
                    if (r0[1] == (p[1] ^ 0x80000000u)) done |= 256u;
                    else if (r0[1] != p[1]) { __hip_atomic_store((gu64*)(wb + O_ACKE + 2048 + tid * 32), ((unsigned long long)fl << 32) | r0[1], RLX_AGENT); p[1] = r0[1]; }
                }
#pragma unroll
                for (int i = 0; i < 8; ++i) {
                    if (!first2 && r[i] == (q[i] ^ 0x80000000u)) { done |= 1u << i; continue; }
                    if (first2 || r[i] != q[i]) __hip_atomic_store((gu64*)(wb + O_ACKL + (i * 64 + tid) * 8), ((unsigned long long)fl << 32) | r[i], RLX_AGENT);
                    q[i] = r[i];
                }
                first2 = false;
                if (__all(done == 511u)) break;
                __builtin_amdgcn_s_sleep(8);
            }
        }
        return 0u;
    }
    volatile __attribute__((address_space(3))) unsigned* sh = (volatile __attribute__((address_space(3))) unsigned*)smem;
    if (tid == 0) {
        const unsigned r0 = ld_dev(wb + O_REQE + e * 32), a0 = ld_dev(wb + O_ACKE + e * 32);
        const unsigned nonce = ((r0 + 1u) ^ a0) & 0x3fffffffu ? r0 + 1u : r0 + 2u;
        st_dev(wb + O_REQE + e * 32, nonce);
        sh[0] = 0u; sh[1] = nonce; sh[4] = sh[5] = 1u << 30;
    }
    bool last;
    unsigned mark = 0x80000000u;
    if (e > 124u) {
        drain();
        __syncthreads();
        const int w = tid >> 6, l = tid & 63, b = e - 125u;
        char* base = wb + (size_t)b * (2u << 20) + (2u << 20) - 16384 - w * (b == 1 ? 65536 : 16384);
        const u32x4 val = {0u, 0u, 0u, 0u};
        for (int rep = 0; rep < PROBES; ++rep) {
            const int bit = (w + rep) & 1;
            char* p = base + bit * 8192 + ((rep >> 1) & 7) * 1024 + 896 + (l & 7) * 16;
            unsigned long long t0, t1;
            asm volatile("s_memtime %0\n\ts_waitcnt lgkmcnt(0)\n\tglobal_store_dwordx4 %3, %2, off sc1\n\ts_waitcnt vmcnt(0)\n\ts_memtime %1\n\ts_waitcnt lgkmcnt(0)"
                         : "=&s"(t0), "=&s"(t1) : "v"(val), "v"(p) : "memory");
            if (l == 0) atomicMin((unsigned*)smem + 4 + bit, (unsigned)(t1 - t0));
        }
        __syncthreads();
        mark |= ((sh[5] < sh[4] ? 1u : 0u) ^ side) << 30;
        last = tid == 0;
    } else if (e >= 80u) {
        const unsigned nw = xq == 3u ? 16u : 24u, u = (rank - 10u) * 4u + wave;
        unsigned sink = 0;
        __builtin_amdgcn_s_sleep(WARM_DELAY);
        for (unsigned i = u; i < WARM_N; i += nw) {
            const unsigned n = i * 64u + (tid & 63), h = n >> 1;
            const char* p = (n & 1u) ? (h < 15360u ? (const char*)Wv + (h / 24u) * 12288u + xq * 3072u + (h % 24u) * 128u : (const char*)Wo + xq * 1966080u + (h - 15360u) * 128u)
                                     : (const char*)(h % 48u < 24u ? Wq : Wk) + (h / 48u) * 12288u + xq * 3072u + (h % 24u) * 128u;
            asm volatile("global_load_dword %0, %1, off" : "+v"(sink) : "v"(p) : "memory");
            if (xq == 3u) __builtin_amdgcn_s_sleep(WARM_NAP * 2 / 3); else __builtin_amdgcn_s_sleep(WARM_NAP);
        }
        asm volatile("s_waitcnt vmcnt(0)" : "+v"(sink) :: "memory");
        last = tid == 0;
    } else {
        u32x4 d0[5], d1[5];
        ln_load(d0, x, e, tid);
        if (e >= 32u) ln_load(d1, x, e + 48, tid);
        const u32x4 s = {~0u, ~0u, ~0u, ~0u}, z = {0u, 0u, 0u, 0u};
        constexpr int NM = 3 * 4 * 384, NP = 16 * 8 * 48, NZ = (int)ZSLOT / 16, NF = (int)(O_FLIP - R_FLAG) / 128, NT = 128, N0 = 640;
        for (int i = e * 256 + tid; i < NM + NP + NZ + NF + NT + N0; i += 80 * 256) {
            int j = i;
            if (j < NZ) { st16_dev(wb + R_Z + (size_t)j * 16, s); continue; }
            j -= NZ;
            if (j < NM) { st16_dev(wb + R_M + (size_t)(j / 384) * 8192 + (j % 384) * 16, s); continue; }
            j -= NM;
            if (j < NP) { st16_dev(wb + R_P + (size_t)(j / 48) * 1024 + (j % 48) * 16, s); continue; }
            j -= NP;
            if (j < NF) { st16_dev(wb + R_FLAG + (size_t)j * 128, z); continue; }
            j -= NF;
            if (j < NT) { st16_dev(wb + O_TK + j * 256, u32x4{j < 64 ? 1u : 0u, 0u, 0u, 0u}); continue; }
            j -= NT;
            const int t = j >> 6, gA = (j >> 4) & 3, r = j & 15;
            const unsigned* src = (const unsigned*)(mem0 + (r >> 1) * 640 + 64 * t + 16 * gA + 2 * (r & 1));
            const u32x4 vv = {min(src[0], 0xfffffffeu), src[2], min(src[4], 0xfffffffeu), src[6]};
            st16_dev(wb + O_M0 + (t < 6 ? t * 1024 : 16384 + (t - 6) * 1024) + (j & 63) * 16, vv);
        }
        ln_tile(d0, wb, e, tid);
        if (e >= 32u) ln_tile(d1, wb, e + 48, tid);
        drain();
        __syncthreads();
        last = tid == 0;
    }
    if (last) {
        const unsigned nonce = sh[1];
        unsigned spins = 0;
        unsigned long long a;
        while ((unsigned)(a = __hip_atomic_load((gu64*)(wb + O_ACKE + e * 32), RLX_AGENT)) != nonce && ++spins < BOOT_SPIN) __builtin_amdgcn_s_sleep(1);
        st_dev(wb + O_REQE + e * 32, nonce ^ mark);
        if (e < 80u || e > 124u)
            while ((unsigned)(a = __hip_atomic_load((gu64*)(wb + O_ACKE + e * 32), RLX_AGENT)) != (nonce ^ mark) && ++spins < BOOT_SPIN) __builtin_amdgcn_s_sleep(2);
        sh[2] = (unsigned)(a >> 32);
    }
    __syncthreads();
    const unsigned fl = __builtin_amdgcn_readfirstlane(sh[2]);
    __syncthreads();
    return fl;
}

__global__ void __launch_bounds__(256, 1) chain_kernel(const u16* Wq, const u16* Wk, const u16* Wv, const u16* Wo, const u16* x, const u16* mem0,
                                                       const u16* w1, const u16* b1, const u16* w2, u16* out, char* wb) {
    extern __shared__ __attribute__((aligned(16))) char smem[];
    const unsigned wave = __builtin_amdgcn_readfirstlane(threadIdx.x >> 6);
    const unsigned side = (blockIdx.x >> 2) & 1u, xq = blockIdx.x & 3u, k = blockIdx.x >> 3;
    const unsigned role = k < 8u ? 0u : k < 16u ? 1u : k < 26u ? 2u : k < 29u ? 3u : 4u;
    const unsigned id = role == 0u ? xq * 8u + k : role == 1u ? xq * 8u + k - 8u : role == 2u ? xq * 10u + k - 16u : role == 3u ? xq * 3u + k - 26u : xq * 3u + k - 29u;
    unsigned flips = 0;
    if (role >= 2u) {
        const unsigned e = role == 2u ? side * 40u + id : role == 3u ? 80u + side * 12u + id : id < 10u ? 104u + side * 10u + id : 124u + side * 2u + id - 10u;
        flips = boot(x, mem0, Wq, Wk, Wv, Wo, wb, e, side, xq, k - 16u, wave, smem);
    }
    const unsigned pk = id | (side << 8) | (wave << 10) | (flips << 12) | ((role == 4u && id >= 10u ? 5u : role) << 16);
    const u16* p0 = role == 0u ? Wq : role == 1u ? Wv : role == 2u ? x : role == 3u ? w1 : w2;
    const u16* p1 = role == 0u ? Wk : role == 1u ? Wo : role == 2u ? x : role == 3u ? b1 : (const u16*)out;
    asm volatile(
"s_lshr_b32 s27, %3, 16\n"
"s_cmp_eq_u32 s27, 0\n"
"s_cbranch_scc1 .Lrole_a\n"
"s_cmp_eq_u32 s27, 1\n"
"s_cbranch_scc1 .Lrole_v\n"
"s_cmp_eq_u32 s27, 2\n"
"s_cbranch_scc1 .Lrole_b\n"
"s_cmp_eq_u32 s27, 3\n"
"s_cbranch_scc1 .Lrole_f\n"
"s_cmp_eq_u32 s27, 4\n"
"s_cbranch_scc1 .Lrole_g\n"
"s_branch .Lrole_end\n"
".Lrole_a:\n"
"s_mov_b64 s[20:21], %2\n"
"s_and_b32 s23, %3, 0xff\n"
"s_bfe_u32 s24, %3, 0x10008\n"
"s_bfe_u32 s22, %3, 0x2000a\n"
"s_bfe_u32 s68, %3, 0x3000c\n"
"s_mov_b32 s26, 0\n"
"s_mov_b32 s25, s24\n"
"s_mov_b64 exec, -1\n"
"s_mov_b32 s40, 0xffffffff\n"
"s_mov_b32 s41, 0x0\n"
"s_mov_b32 s42, 0xff00ff00\n"
"s_mov_b32 s43, 0xff00ff00\n"
"s_mov_b32 s44, 0x55555555\n"
"s_mov_b32 s45, 0x55555555\n"
"v_mbcnt_lo_u32_b32 v10, -1, 0\n"
"v_mbcnt_hi_u32_b32 v10, -1, v10\n"
"v_lshlrev_b32 v0, 4, v10\n"
"v_mov_b32 v11, 0x4444\n"
"v_mov_b32 v12, 0xeeee\n"
"v_cndmask_b32 v1, v12, v11, s[44:45]\n"
"s_getpc_b64 s[28:29]\n"
".La_pc:\n"
"s_and_b32 s28, s28, 0xffffff80\n"
"v_lshlrev_b32 v101, 3, v0\n"
"s_lshl_b32 s27, s22, 13\n"
"v_add_u32 v101, s27, v101\n"
"v_min_u32 v101, .La_end-.La_pc-4, v101\n"
"global_load_dword v101, v101, s[28:29]\n"
"s_mov_b32 s69, 0x3dd53b95\n"
"s_mov_b32 s71, 0xbdd53b95\n"
"s_mov_b32 s48, 0xfff0ffff\n"
"s_mov_b32 s49, 0xf000ff00\n"
"s_mov_b32 s50, 0xffe0fffe\n"
"s_mov_b32 s51, 0xe000fe00\n"
"s_mov_b32 s52, 0xffc0fffc\n"
"s_mov_b32 s53, 0xc000fc00\n"
"s_mov_b32 s54, 0xff80fff8\n"
"s_mov_b32 s55, 0x8000f800\n"
"s_cmp_lt_u32 s22, 2\n"
"s_cselect_b64 s[58:59], %0, %1\n"
"s_mul_i32 s27, s23, 384\n"
"s_add_u32 s58, s58, s27\n"
"s_addc_u32 s59, s59, 0\n"
"s_and_b32 s27, s22, 1\n"
"s_mul_i32 s28, s27, 384\n"
"s_add_u32 s28, s28, 0\n"
"v_add_u32 v11, s28, v10\n"
"v_mul_u32_u24 v12, 0xaaab, v11\n"
"v_lshrrev_b32 v12, 20, v12\n"
"v_mul_u32_u24 v13, 24, v12\n"
"v_sub_u32 v13, v11, v13\n"
"v_and_b32 v14, 2, v12\n"
"v_lshrrev_b32 v15, 1, v12\n"
"v_and_b32 v15, 4, v15\n"
"v_or_b32 v14, v14, v15\n"
"v_xor_b32 v13, v13, v14\n"
"v_mul_u32_u24 v12, 0x3000, v12\n"
"v_lshl_add_u32 v86, v13, 4, v12\n"
"s_mul_i32 s28, s27, 384\n"
"s_add_u32 s28, s28, 64\n"
"v_add_u32 v11, s28, v10\n"
"v_mul_u32_u24 v12, 0xaaab, v11\n"
"v_lshrrev_b32 v12, 20, v12\n"
"v_mul_u32_u24 v13, 24, v12\n"
"v_sub_u32 v13, v11, v13\n"
"v_and_b32 v14, 2, v12\n"
"v_lshrrev_b32 v15, 1, v12\n"
"v_and_b32 v15, 4, v15\n"
"v_or_b32 v14, v14, v15\n"
"v_xor_b32 v13, v13, v14\n"
"v_mul_u32_u24 v12, 0x3000, v12\n"
"v_lshl_add_u32 v87, v13, 4, v12\n"
"s_mul_i32 s28, s27, 384\n"
"s_add_u32 s28, s28, 128\n"
"v_add_u32 v11, s28, v10\n"
"v_mul_u32_u24 v12, 0xaaab, v11\n"
"v_lshrrev_b32 v12, 20, v12\n"
"v_mul_u32_u24 v13, 24, v12\n"
"v_sub_u32 v13, v11, v13\n"
"v_and_b32 v14, 2, v12\n"
"v_lshrrev_b32 v15, 1, v12\n"
"v_and_b32 v15, 4, v15\n"
"v_or_b32 v14, v14, v15\n"
"v_xor_b32 v13, v13, v14\n"
"v_mul_u32_u24 v12, 0x3000, v12\n"
"v_lshl_add_u32 v88, v13, 4, v12\n"
"s_mul_i32 s28, s27, 384\n"
"s_add_u32 s28, s28, 192\n"
"v_add_u32 v11, s28, v10\n"
"v_mul_u32_u24 v12, 0xaaab, v11\n"
"v_lshrrev_b32 v12, 20, v12\n"
"v_mul_u32_u24 v13, 24, v12\n"
"v_sub_u32 v13, v11, v13\n"
"v_and_b32 v14, 2, v12\n"
"v_lshrrev_b32 v15, 1, v12\n"
"v_and_b32 v15, 4, v15\n"
"v_or_b32 v14, v14, v15\n"
"v_xor_b32 v13, v13, v14\n"
"v_mul_u32_u24 v12, 0x3000, v12\n"
"v_lshl_add_u32 v89, v13, 4, v12\n"
"s_mul_i32 s28, s27, 384\n"
"s_add_u32 s28, s28, 256\n"
"v_add_u32 v11, s28, v10\n"
"v_mul_u32_u24 v12, 0xaaab, v11\n"
"v_lshrrev_b32 v12, 20, v12\n"
"v_mul_u32_u24 v13, 24, v12\n"
"v_sub_u32 v13, v11, v13\n"
"v_and_b32 v14, 2, v12\n"
"v_lshrrev_b32 v15, 1, v12\n"
"v_and_b32 v15, 4, v15\n"
"v_or_b32 v14, v14, v15\n"
"v_xor_b32 v13, v13, v14\n"
"v_mul_u32_u24 v12, 0x3000, v12\n"
"v_lshl_add_u32 v90, v13, 4, v12\n"
"s_mul_i32 s28, s27, 384\n"
"s_add_u32 s28, s28, 320\n"
"v_add_u32 v11, s28, v10\n"
"v_mul_u32_u24 v12, 0xaaab, v11\n"
"v_lshrrev_b32 v12, 20, v12\n"
"v_mul_u32_u24 v13, 24, v12\n"
"v_sub_u32 v13, v11, v13\n"
"v_and_b32 v14, 2, v12\n"
"v_lshrrev_b32 v15, 1, v12\n"
"v_and_b32 v15, 4, v15\n"
"v_or_b32 v14, v14, v15\n"
"v_xor_b32 v13, v13, v14\n"
"v_mul_u32_u24 v12, 0x3000, v12\n"
"v_lshl_add_u32 v91, v13, 4, v12\n"
"s_lshr_b32 s70, s22, 1\n"
"s_mul_i32 s70, s70, 12288\n"
"v_lshrrev_b32 v11, 4, v10\n"
"v_lshlrev_b32 v11, 3, v11\n"
"v_bfe_u32 v12, v10, 2, 2\n"
"v_add_u32 v11, v11, v12\n"
"v_and_b32 v14, 2, v11\n"
"v_lshrrev_b32 v15, 1, v11\n"
"v_and_b32 v15, 4, v15\n"
"v_or_b32 v14, v14, v15\n"
"v_mul_u32_u24 v11, 384, v11\n"
"v_and_b32 v12, 1, v10\n"
"v_lshl_add_u32 v11, v12, 3, v11\n"
"v_add_u32 v11, s70, v11\n"
"v_bfe_u32 v12, v10, 1, 1\n"
"s_mul_i32 s28, s27, 12\n"
"s_add_u32 s28, s28, 0\n"
"v_add_u32 v13, s28, v12\n"
"v_xor_b32 v13, v13, v14\n"
"v_lshl_add_u32 v92, v13, 4, v11\n"
"s_mul_i32 s28, s27, 12\n"
"s_add_u32 s28, s28, 2\n"
"v_add_u32 v13, s28, v12\n"
"v_xor_b32 v13, v13, v14\n"
"v_lshl_add_u32 v93, v13, 4, v11\n"
"s_mul_i32 s28, s27, 12\n"
"s_add_u32 s28, s28, 4\n"
"v_add_u32 v13, s28, v12\n"
"v_xor_b32 v13, v13, v14\n"
"v_lshl_add_u32 v94, v13, 4, v11\n"
"s_mul_i32 s28, s27, 12\n"
"s_add_u32 s28, s28, 6\n"
"v_add_u32 v13, s28, v12\n"
"v_xor_b32 v13, v13, v14\n"
"v_lshl_add_u32 v95, v13, 4, v11\n"
"s_mul_i32 s28, s27, 12\n"
"s_add_u32 s28, s28, 8\n"
"v_add_u32 v13, s28, v12\n"
"v_xor_b32 v13, v13, v14\n"
"v_lshl_add_u32 v96, v13, 4, v11\n"
"s_mul_i32 s28, s27, 12\n"
"s_add_u32 s28, s28, 10\n"
"v_add_u32 v13, s28, v12\n"
"v_xor_b32 v13, v13, v14\n"
"v_lshl_add_u32 v97, v13, 4, v11\n"
"v_and_b32 v11, 15, v10\n"
"v_lshlrev_b32 v11, 4, v11\n"
"v_lshrrev_b32 v12, 4, v10\n"
"v_lshl_add_u32 v11, v12, 2, v11\n"
"s_mul_i32 s27, s22, 1536\n"
"s_add_u32 s27, s27, 149504\n"
"v_add_u32 v2, s27, v11\n"
"v_lshrrev_b32 v11, 2, v10\n"
"v_lshlrev_b32 v11, 4, v11\n"
"v_and_b32 v12, 1, v10\n"
"v_lshl_add_u32 v11, v12, 3, v11\n"
"v_add_u32 v3, 0x24800, v11\n"
"s_mul_i32 s27, s22, 18432\n"
"v_add_u32 v4, s27, v0\n"
"v_add_u32 v5, 0x12000, v0\n"
"v_add_u32 v6, 0x22000, v0\n"
"v_and_b32 v11, 15, v10\n"
"v_lshlrev_b32 v11, 4, v11\n"
"v_bfe_u32 v12, v10, 4, 1\n"
"v_lshl_add_u32 v11, v12, 3, v11\n"
"v_and_b32 v13, 7, v10\n"
"v_lshlrev_b32 v13, 5, v13\n"
"v_bfe_u32 v12, v10, 3, 1\n"
"v_lshl_add_u32 v13, v12, 4, v13\n"
"v_bfe_u32 v12, v10, 4, 1\n"
"v_lshl_add_u32 v13, v12, 3, v13\n"
"v_lshrrev_b32 v12, 5, v10\n"
"v_lshl_add_u32 v7, v12, 8, v13\n"
"s_and_b32 s27, s23, 7\n"
"s_mul_i32 s27, s27, 768\n"
"s_sub_u32 s27, 8192, s27\n"
"v_add_u32 v13, s27, v11\n"
"v_and_b32 v14, 7, v10\n"
"v_lshlrev_b32 v14, 5, v14\n"
"v_bfe_u32 v12, v10, 3, 1\n"
"v_lshl_add_u32 v14, v12, 4, v14\n"
"v_bfe_u32 v12, v10, 4, 1\n"
"v_lshl_add_u32 v14, v12, 2, v14\n"
"s_and_b32 s27, s23, 1\n"
"s_mul_i32 s27, s27, 1016\n"
"s_sub_u32 s27, 1536, s27\n"
"v_add_u32 v14, s27, v14\n"
"s_cmp_eq_u32 s22, 0\n"
"s_cselect_b64 vcc, -1, 0\n"
"v_cndmask_b32 v8, v13, v14, vcc\n"
"s_lshl_b32 s27, s24, 5\n"
"s_add_u32 s27, s27, s23\n"
"s_add_u32 s27, s27, 0\n"
"s_lshl_b32 s27, s27, 2\n"
"s_add_u32 s27, s27, s22\n"
"s_lshl_b32 s27, s27, 2\n"
"s_add_u32 s28, s27, 0x622000\n"
"s_add_u32 s90, s20, s28\n"
"s_addc_u32 s91, s21, 0\n"
"s_lshl_b32 s27, s27, 1\n"
"s_add_u32 s27, s27, 0x623000\n"
"s_add_u32 s92, s20, s27\n"
"s_addc_u32 s93, s21, 0\n"
"v_mov_b32 v9, 0\n"
"global_load_dword v10, v9, s[90:91] sc1\n"
"global_load_dword v11, v9, s[92:93] sc1\n"
"s_mul_i32 s27, s22, 6144\n"
"s_add_u32 s27, s27, 0\n"
"s_mov_b32 m0, s27\n"
"s_add_u32 s27, s27, 1024\n"
"global_load_lds_dwordx4 v86, s[58:59]\n"
"s_mov_b32 m0, s27\n"
"s_add_u32 s27, s27, 1024\n"
"global_load_lds_dwordx4 v87, s[58:59]\n"
"s_mov_b32 m0, s27\n"
"s_add_u32 s27, s27, 1024\n"
"global_load_lds_dwordx4 v88, s[58:59]\n"
"s_mov_b32 m0, s27\n"
"s_add_u32 s27, s27, 1024\n"
"global_load_lds_dwordx4 v89, s[58:59]\n"
"s_mov_b32 m0, s27\n"
"s_add_u32 s27, s27, 1024\n"
"global_load_lds_dwordx4 v90, s[58:59]\n"
"s_mov_b32 m0, s27\n"
"s_nop 0\n"
"global_load_lds_dwordx4 v91, s[58:59]\n"
"s_add_u32 s58, s58, 0x60000\n"
"s_addc_u32 s59, s59, 0\n"
"s_mul_i32 s27, s22, 6144\n"
"s_add_u32 s27, s27, 24576\n"
"s_mov_b32 m0, s27\n"
"s_add_u32 s27, s27, 1024\n"
"global_load_lds_dwordx4 v86, s[58:59]\n"
"s_mov_b32 m0, s27\n"
"s_add_u32 s27, s27, 1024\n"
"global_load_lds_dwordx4 v87, s[58:59]\n"
"s_mov_b32 m0, s27\n"
"s_add_u32 s27, s27, 1024\n"
"global_load_lds_dwordx4 v88, s[58:59]\n"
"s_mov_b32 m0, s27\n"
"s_add_u32 s27, s27, 1024\n"
"global_load_lds_dwordx4 v89, s[58:59]\n"
"s_mov_b32 m0, s27\n"
"s_add_u32 s27, s27, 1024\n"
"global_load_lds_dwordx4 v90, s[58:59]\n"
"s_mov_b32 m0, s27\n"
"s_nop 0\n"
"global_load_lds_dwordx4 v91, s[58:59]\n"
"s_add_u32 s58, s58, 0x60000\n"
"s_addc_u32 s59, s59, 0\n"
"s_mul_i32 s27, s22, 6144\n"
"s_add_u32 s27, s27, 49152\n"
"s_mov_b32 m0, s27\n"
"s_add_u32 s27, s27, 1024\n"
"global_load_lds_dwordx4 v86, s[58:59]\n"
"s_mov_b32 m0, s27\n"
"s_add_u32 s27, s27, 1024\n"
"global_load_lds_dwordx4 v87, s[58:59]\n"
"s_mov_b32 m0, s27\n"
"s_add_u32 s27, s27, 1024\n"
"global_load_lds_dwordx4 v88, s[58:59]\n"
"s_mov_b32 m0, s27\n"
"s_add_u32 s27, s27, 1024\n"
"global_load_lds_dwordx4 v89, s[58:59]\n"
"s_mov_b32 m0, s27\n"
"s_add_u32 s27, s27, 1024\n"
"global_load_lds_dwordx4 v90, s[58:59]\n"
"s_mov_b32 m0, s27\n"
"s_nop 0\n"
"global_load_lds_dwordx4 v91, s[58:59]\n"
"s_add_u32 s58, s58, 0x60000\n"
"s_addc_u32 s59, s59, 0\n"
"s_mul_i32 s27, s22, 6144\n"
"s_add_u32 s27, s27, 114688\n"
"s_mov_b32 m0, s27\n"
"s_add_u32 s27, s27, 1024\n"
"global_load_lds_dwordx4 v86, s[58:59]\n"
"s_mov_b32 m0, s27\n"
"s_add_u32 s27, s27, 1024\n"
"global_load_lds_dwordx4 v87, s[58:59]\n"
"s_mov_b32 m0, s27\n"
"s_add_u32 s27, s27, 1024\n"
"global_load_lds_dwordx4 v88, s[58:59]\n"
"s_mov_b32 m0, s27\n"
"s_add_u32 s27, s27, 1024\n"
"global_load_lds_dwordx4 v89, s[58:59]\n"
"s_mov_b32 m0, s27\n"
"s_add_u32 s27, s27, 1024\n"
"global_load_lds_dwordx4 v90, s[58:59]\n"
"s_mov_b32 m0, s27\n"
"s_nop 0\n"
"global_load_lds_dwordx4 v91, s[58:59]\n"
"s_add_u32 s58, s58, 0x60000\n"
"s_addc_u32 s59, s59, 0\n"
"s_waitcnt vmcnt(18)\n"
"s_waitcnt lgkmcnt(0)\n"
"s_barrier\n"
"v_add_u32 v84, 1, v10\n"
"v_add_u32 v10, 2, v10\n"
"v_cmp_eq_u32 vcc, v84, v11\n"
"s_nop 1\n"
"v_cndmask_b32 v84, v84, v10, vcc\n"
"s_mov_b64 exec, 1\n"
"global_store_dword v9, v84, s[90:91] sc1\n"
"s_mov_b64 exec, -1\n"
"s_mul_i32 s27, s22, 6144\n"
"s_add_u32 s27, s27, 139264\n"
"s_mov_b32 m0, s27\n"
"s_add_u32 s27, s27, 1024\n"
"global_load_lds_dwordx4 v86, s[58:59]\n"
"s_mov_b32 m0, s27\n"
"s_add_u32 s27, s27, 1024\n"
"global_load_lds_dwordx4 v87, s[58:59]\n"
"s_mov_b32 m0, s27\n"
"s_add_u32 s27, s27, 1024\n"
"global_load_lds_dwordx4 v88, s[58:59]\n"
"s_mov_b32 m0, s27\n"
"s_add_u32 s27, s27, 1024\n"
"global_load_lds_dwordx4 v89, s[58:59]\n"
"s_mov_b32 m0, s27\n"
"s_add_u32 s27, s27, 1024\n"
"global_load_lds_dwordx4 v90, s[58:59]\n"
"s_mov_b32 m0, s27\n"
"s_nop 0\n"
"global_load_lds_dwordx4 v91, s[58:59]\n"
"s_add_u32 s58, s58, 0x60000\n"
"s_addc_u32 s59, s59, 0\n"
"ds_read_b64_tr_b16 a[0:1], v92 offset:0\n"
"ds_read_b64_tr_b16 a[2:3], v92 offset:1536\n"
"ds_read_b64_tr_b16 a[8:9], v93 offset:0\n"
"ds_read_b64_tr_b16 a[10:11], v93 offset:1536\n"
"ds_read_b64_tr_b16 a[16:17], v94 offset:0\n"
"ds_read_b64_tr_b16 a[18:19], v94 offset:1536\n"
"ds_read_b64_tr_b16 a[24:25], v95 offset:0\n"
"ds_read_b64_tr_b16 a[26:27], v95 offset:1536\n"
"ds_read_b64_tr_b16 a[32:33], v96 offset:0\n"
"ds_read_b64_tr_b16 a[34:35], v96 offset:1536\n"
"ds_read_b64_tr_b16 a[40:41], v97 offset:0\n"
"ds_read_b64_tr_b16 a[42:43], v97 offset:1536\n"
"s_waitcnt vmcnt(18)\n"
"s_waitcnt lgkmcnt(0)\n"
"s_barrier\n"
"s_mul_i32 s27, s22, 6144\n"
"s_add_u32 s27, s27, 0\n"
"s_mov_b32 m0, s27\n"
"s_add_u32 s27, s27, 1024\n"
"global_load_lds_dwordx4 v86, s[58:59]\n"
"s_mov_b32 m0, s27\n"
"s_add_u32 s27, s27, 1024\n"
"global_load_lds_dwordx4 v87, s[58:59]\n"
"s_mov_b32 m0, s27\n"
"s_add_u32 s27, s27, 1024\n"
"global_load_lds_dwordx4 v88, s[58:59]\n"
"s_mov_b32 m0, s27\n"
"s_add_u32 s27, s27, 1024\n"
"global_load_lds_dwordx4 v89, s[58:59]\n"
"s_mov_b32 m0, s27\n"
"s_add_u32 s27, s27, 1024\n"
"global_load_lds_dwordx4 v90, s[58:59]\n"
"s_mov_b32 m0, s27\n"
"s_nop 0\n"
"global_load_lds_dwordx4 v91, s[58:59]\n"
"s_add_u32 s58, s58, 0x60000\n"
"s_addc_u32 s59, s59, 0\n"
"ds_read_b64_tr_b16 a[4:5], v92 offset:24576\n"
"ds_read_b64_tr_b16 a[6:7], v92 offset:26112\n"
"ds_read_b64_tr_b16 a[12:13], v93 offset:24576\n"
"ds_read_b64_tr_b16 a[14:15], v93 offset:26112\n"
"ds_read_b64_tr_b16 a[20:21], v94 offset:24576\n"
"ds_read_b64_tr_b16 a[22:23], v94 offset:26112\n"
"ds_read_b64_tr_b16 a[28:29], v95 offset:24576\n"
"ds_read_b64_tr_b16 a[30:31], v95 offset:26112\n"
"ds_read_b64_tr_b16 a[36:37], v96 offset:24576\n"
"ds_read_b64_tr_b16 a[38:39], v96 offset:26112\n"
"ds_read_b64_tr_b16 a[44:45], v97 offset:24576\n"
"ds_read_b64_tr_b16 a[46:47], v97 offset:26112\n"
"s_waitcnt vmcnt(18)\n"
"s_waitcnt lgkmcnt(0)\n"
"s_barrier\n"
"s_mul_i32 s27, s22, 6144\n"
"s_add_u32 s27, s27, 24576\n"
"s_mov_b32 m0, s27\n"
"s_add_u32 s27, s27, 1024\n"
"global_load_lds_dwordx4 v86, s[58:59]\n"
"s_mov_b32 m0, s27\n"
"s_add_u32 s27, s27, 1024\n"
"global_load_lds_dwordx4 v87, s[58:59]\n"
"s_mov_b32 m0, s27\n"
"s_add_u32 s27, s27, 1024\n"
"global_load_lds_dwordx4 v88, s[58:59]\n"
"s_mov_b32 m0, s27\n"
"s_add_u32 s27, s27, 1024\n"
"global_load_lds_dwordx4 v89, s[58:59]\n"
"s_mov_b32 m0, s27\n"
"s_add_u32 s27, s27, 1024\n"
"global_load_lds_dwordx4 v90, s[58:59]\n"
"s_mov_b32 m0, s27\n"
"s_nop 0\n"
"global_load_lds_dwordx4 v91, s[58:59]\n"
"s_add_u32 s58, s58, 0x60000\n"
"s_addc_u32 s59, s59, 0\n"
"ds_read_b64_tr_b16 a[48:49], v92 offset:49152\n"
"ds_read_b64_tr_b16 a[50:51], v92 offset:50688\n"
"ds_read_b64_tr_b16 a[56:57], v93 offset:49152\n"
"ds_read_b64_tr_b16 a[58:59], v93 offset:50688\n"
"ds_read_b64_tr_b16 a[64:65], v94 offset:49152\n"
"ds_read_b64_tr_b16 a[66:67], v94 offset:50688\n"
"ds_read_b64_tr_b16 a[72:73], v95 offset:49152\n"
"ds_read_b64_tr_b16 a[74:75], v95 offset:50688\n"
"ds_read_b64_tr_b16 a[80:81], v96 offset:49152\n"
"ds_read_b64_tr_b16 a[82:83], v96 offset:50688\n"
"ds_read_b64_tr_b16 a[88:89], v97 offset:49152\n"
"ds_read_b64_tr_b16 a[90:91], v97 offset:50688\n"
"s_waitcnt vmcnt(18)\n"
"s_waitcnt lgkmcnt(0)\n"
"s_barrier\n"
"s_mul_i32 s27, s22, 6144\n"
"s_add_u32 s27, s27, 49152\n"
"s_mov_b32 m0, s27\n"
"s_add_u32 s27, s27, 1024\n"
"global_load_lds_dwordx4 v86, s[58:59]\n"
"s_mov_b32 m0, s27\n"
"s_add_u32 s27, s27, 1024\n"
"global_load_lds_dwordx4 v87, s[58:59]\n"
"s_mov_b32 m0, s27\n"
"s_add_u32 s27, s27, 1024\n"
"global_load_lds_dwordx4 v88, s[58:59]\n"
"s_mov_b32 m0, s27\n"
"s_add_u32 s27, s27, 1024\n"
"global_load_lds_dwordx4 v89, s[58:59]\n"
"s_mov_b32 m0, s27\n"
"s_add_u32 s27, s27, 1024\n"
"global_load_lds_dwordx4 v90, s[58:59]\n"
"s_mov_b32 m0, s27\n"
"s_nop 0\n"
"global_load_lds_dwordx4 v91, s[58:59]\n"
"s_add_u32 s58, s58, 0x60000\n"
"s_addc_u32 s59, s59, 0\n"
"v_add_u32 v92, 0x1c000, v92\n"
"v_add_u32 v93, 0x1c000, v93\n"
"v_add_u32 v94, 0x1c000, v94\n"
"v_add_u32 v95, 0x1c000, v95\n"
"v_add_u32 v96, 0x1c000, v96\n"
"v_add_u32 v97, 0x1c000, v97\n"
"ds_read_b64_tr_b16 a[52:53], v92 offset:0\n"
"ds_read_b64_tr_b16 a[54:55], v92 offset:1536\n"
"ds_read_b64_tr_b16 a[60:61], v93 offset:0\n"
"ds_read_b64_tr_b16 a[62:63], v93 offset:1536\n"
"ds_read_b64_tr_b16 a[68:69], v94 offset:0\n"
"ds_read_b64_tr_b16 a[70:71], v94 offset:1536\n"
"ds_read_b64_tr_b16 a[76:77], v95 offset:0\n"
"ds_read_b64_tr_b16 a[78:79], v95 offset:1536\n"
"ds_read_b64_tr_b16 a[84:85], v96 offset:0\n"
"ds_read_b64_tr_b16 a[86:87], v96 offset:1536\n"
"ds_read_b64_tr_b16 a[92:93], v97 offset:0\n"
"ds_read_b64_tr_b16 a[94:95], v97 offset:1536\n"
"s_waitcnt vmcnt(18)\n"
"s_waitcnt lgkmcnt(0)\n"
"s_barrier\n"
"s_mul_i32 s27, s22, 6144\n"
"s_add_u32 s27, s27, 114688\n"
"s_mov_b32 m0, s27\n"
"s_add_u32 s27, s27, 1024\n"
"global_load_lds_dwordx4 v86, s[58:59]\n"
"s_mov_b32 m0, s27\n"
"s_add_u32 s27, s27, 1024\n"
"global_load_lds_dwordx4 v87, s[58:59]\n"
"s_mov_b32 m0, s27\n"
"s_add_u32 s27, s27, 1024\n"
"global_load_lds_dwordx4 v88, s[58:59]\n"
"s_mov_b32 m0, s27\n"
"s_add_u32 s27, s27, 1024\n"
"global_load_lds_dwordx4 v89, s[58:59]\n"
"s_mov_b32 m0, s27\n"
"s_add_u32 s27, s27, 1024\n"
"global_load_lds_dwordx4 v90, s[58:59]\n"
"s_mov_b32 m0, s27\n"
"s_nop 0\n"
"global_load_lds_dwordx4 v91, s[58:59]\n"
"s_add_u32 s58, s58, 0x60000\n"
"s_addc_u32 s59, s59, 0\n"
"ds_read_b64_tr_b16 a[96:97], v92 offset:24576\n"
"ds_read_b64_tr_b16 a[98:99], v92 offset:26112\n"
"ds_read_b64_tr_b16 a[104:105], v93 offset:24576\n"
"ds_read_b64_tr_b16 a[106:107], v93 offset:26112\n"
"ds_read_b64_tr_b16 a[112:113], v94 offset:24576\n"
"ds_read_b64_tr_b16 a[114:115], v94 offset:26112\n"
"ds_read_b64_tr_b16 a[120:121], v95 offset:24576\n"
"ds_read_b64_tr_b16 a[122:123], v95 offset:26112\n"
"ds_read_b64_tr_b16 a[128:129], v96 offset:24576\n"
"ds_read_b64_tr_b16 a[130:131], v96 offset:26112\n"
"ds_read_b64_tr_b16 a[136:137], v97 offset:24576\n"
"ds_read_b64_tr_b16 a[138:139], v97 offset:26112\n"
"s_waitcnt vmcnt(18)\n"
"s_waitcnt lgkmcnt(0)\n"
"s_barrier\n"
"s_mul_i32 s27, s22, 6144\n"
"s_add_u32 s27, s27, 139264\n"
"s_mov_b32 m0, s27\n"
"s_add_u32 s27, s27, 1024\n"
"global_load_lds_dwordx4 v86, s[58:59]\n"
"s_mov_b32 m0, s27\n"
"s_add_u32 s27, s27, 1024\n"
"global_load_lds_dwordx4 v87, s[58:59]\n"
"s_mov_b32 m0, s27\n"
"s_add_u32 s27, s27, 1024\n"
"global_load_lds_dwordx4 v88, s[58:59]\n"
"s_mov_b32 m0, s27\n"
"s_add_u32 s27, s27, 1024\n"
"global_load_lds_dwordx4 v89, s[58:59]\n"
"s_mov_b32 m0, s27\n"
"s_add_u32 s27, s27, 1024\n"
"global_load_lds_dwordx4 v90, s[58:59]\n"
"s_mov_b32 m0, s27\n"
"s_nop 0\n"
"global_load_lds_dwordx4 v91, s[58:59]\n"
"s_add_u32 s58, s58, 0x60000\n"
"s_addc_u32 s59, s59, 0\n"
"v_subrev_u32 v92, 0x1c000, v92\n"
"v_subrev_u32 v93, 0x1c000, v93\n"
"v_subrev_u32 v94, 0x1c000, v94\n"
"v_subrev_u32 v95, 0x1c000, v95\n"
"v_subrev_u32 v96, 0x1c000, v96\n"
"v_subrev_u32 v97, 0x1c000, v97\n"
"ds_read_b64_tr_b16 a[100:101], v92 offset:0\n"
"ds_read_b64_tr_b16 a[102:103], v92 offset:1536\n"
"ds_read_b64_tr_b16 a[108:109], v93 offset:0\n"
"ds_read_b64_tr_b16 a[110:111], v93 offset:1536\n"
"ds_read_b64_tr_b16 a[116:117], v94 offset:0\n"
"ds_read_b64_tr_b16 a[118:119], v94 offset:1536\n"
"ds_read_b64_tr_b16 a[124:125], v95 offset:0\n"
"ds_read_b64_tr_b16 a[126:127], v95 offset:1536\n"
"ds_read_b64_tr_b16 a[132:133], v96 offset:0\n"
"ds_read_b64_tr_b16 a[134:135], v96 offset:1536\n"
"ds_read_b64_tr_b16 a[140:141], v97 offset:0\n"
"ds_read_b64_tr_b16 a[142:143], v97 offset:1536\n"
"s_waitcnt vmcnt(18)\n"
"s_waitcnt lgkmcnt(0)\n"
"s_barrier\n"
"s_mul_i32 s27, s22, 6144\n"
"s_add_u32 s27, s27, 0\n"
"s_mov_b32 m0, s27\n"
"s_add_u32 s27, s27, 1024\n"
"global_load_lds_dwordx4 v86, s[58:59]\n"
"s_mov_b32 m0, s27\n"
"s_add_u32 s27, s27, 1024\n"
"global_load_lds_dwordx4 v87, s[58:59]\n"
"s_mov_b32 m0, s27\n"
"s_add_u32 s27, s27, 1024\n"
"global_load_lds_dwordx4 v88, s[58:59]\n"
"s_mov_b32 m0, s27\n"
"s_add_u32 s27, s27, 1024\n"
"global_load_lds_dwordx4 v89, s[58:59]\n"
"s_mov_b32 m0, s27\n"
"s_add_u32 s27, s27, 1024\n"
"global_load_lds_dwordx4 v90, s[58:59]\n"
"s_mov_b32 m0, s27\n"
"s_nop 0\n"
"global_load_lds_dwordx4 v91, s[58:59]\n"
"s_add_u32 s58, s58, 0x60000\n"
"s_addc_u32 s59, s59, 0\n"
"ds_read_b64_tr_b16 a[144:145], v92 offset:24576\n"
"ds_read_b64_tr_b16 a[146:147], v92 offset:26112\n"
"ds_read_b64_tr_b16 a[152:153], v93 offset:24576\n"
"ds_read_b64_tr_b16 a[154:155], v93 offset:26112\n"
"ds_read_b64_tr_b16 a[160:161], v94 offset:24576\n"
"ds_read_b64_tr_b16 a[162:163], v94 offset:26112\n"
"ds_read_b64_tr_b16 a[168:169], v95 offset:24576\n"
"ds_read_b64_tr_b16 a[170:171], v95 offset:26112\n"
"ds_read_b64_tr_b16 a[176:177], v96 offset:24576\n"
"ds_read_b64_tr_b16 a[178:179], v96 offset:26112\n"
"ds_read_b64_tr_b16 a[184:185], v97 offset:24576\n"
"ds_read_b64_tr_b16 a[186:187], v97 offset:26112\n"
"s_waitcnt vmcnt(18)\n"
"s_waitcnt lgkmcnt(0)\n"
"s_barrier\n"
"s_mul_i32 s27, s22, 6144\n"
"s_add_u32 s27, s27, 24576\n"
"s_mov_b32 m0, s27\n"
"s_add_u32 s27, s27, 1024\n"
"global_load_lds_dwordx4 v86, s[58:59]\n"
"s_mov_b32 m0, s27\n"
"s_add_u32 s27, s27, 1024\n"
"global_load_lds_dwordx4 v87, s[58:59]\n"
"s_mov_b32 m0, s27\n"
"s_add_u32 s27, s27, 1024\n"
"global_load_lds_dwordx4 v88, s[58:59]\n"
"s_mov_b32 m0, s27\n"
"s_add_u32 s27, s27, 1024\n"
"global_load_lds_dwordx4 v89, s[58:59]\n"
"s_mov_b32 m0, s27\n"
"s_add_u32 s27, s27, 1024\n"
"global_load_lds_dwordx4 v90, s[58:59]\n"
"s_mov_b32 m0, s27\n"
"s_nop 0\n"
"global_load_lds_dwordx4 v91, s[58:59]\n"
"s_add_u32 s58, s58, 0x60000\n"
"s_addc_u32 s59, s59, 0\n"
"ds_read_b64_tr_b16 a[148:149], v92 offset:49152\n"
"ds_read_b64_tr_b16 a[150:151], v92 offset:50688\n"
"ds_read_b64_tr_b16 a[156:157], v93 offset:49152\n"
"ds_read_b64_tr_b16 a[158:159], v93 offset:50688\n"
"ds_read_b64_tr_b16 a[164:165], v94 offset:49152\n"
"ds_read_b64_tr_b16 a[166:167], v94 offset:50688\n"
"ds_read_b64_tr_b16 a[172:173], v95 offset:49152\n"
"ds_read_b64_tr_b16 a[174:175], v95 offset:50688\n"
"ds_read_b64_tr_b16 a[180:181], v96 offset:49152\n"
"ds_read_b64_tr_b16 a[182:183], v96 offset:50688\n"
"ds_read_b64_tr_b16 a[188:189], v97 offset:49152\n"
"ds_read_b64_tr_b16 a[190:191], v97 offset:50688\n"
"s_waitcnt vmcnt(18)\n"
"s_waitcnt lgkmcnt(0)\n"
"s_barrier\n"
"s_mul_i32 s27, s22, 6144\n"
"s_add_u32 s27, s27, 49152\n"
"s_mov_b32 m0, s27\n"
"s_add_u32 s27, s27, 1024\n"
"global_load_lds_dwordx4 v86, s[58:59]\n"
"s_mov_b32 m0, s27\n"
"s_add_u32 s27, s27, 1024\n"
"global_load_lds_dwordx4 v87, s[58:59]\n"
"s_mov_b32 m0, s27\n"
"s_add_u32 s27, s27, 1024\n"
"global_load_lds_dwordx4 v88, s[58:59]\n"
"s_mov_b32 m0, s27\n"
"s_add_u32 s27, s27, 1024\n"
"global_load_lds_dwordx4 v89, s[58:59]\n"
"s_mov_b32 m0, s27\n"
"s_add_u32 s27, s27, 1024\n"
"global_load_lds_dwordx4 v90, s[58:59]\n"
"s_mov_b32 m0, s27\n"
"s_nop 0\n"
"global_load_lds_dwordx4 v91, s[58:59]\n"
"s_add_u32 s58, s58, 0x60000\n"
"s_addc_u32 s59, s59, 0\n"
"v_add_u32 v92, 0x1c000, v92\n"
"v_add_u32 v93, 0x1c000, v93\n"
"v_add_u32 v94, 0x1c000, v94\n"
"v_add_u32 v95, 0x1c000, v95\n"
"v_add_u32 v96, 0x1c000, v96\n"
"v_add_u32 v97, 0x1c000, v97\n"
"ds_read_b64_tr_b16 a[192:193], v92 offset:0\n"
"ds_read_b64_tr_b16 a[194:195], v92 offset:1536\n"
"ds_read_b64_tr_b16 a[200:201], v93 offset:0\n"
"ds_read_b64_tr_b16 a[202:203], v93 offset:1536\n"
"ds_read_b64_tr_b16 a[208:209], v94 offset:0\n"
"ds_read_b64_tr_b16 a[210:211], v94 offset:1536\n"
"ds_read_b64_tr_b16 a[216:217], v95 offset:0\n"
"ds_read_b64_tr_b16 a[218:219], v95 offset:1536\n"
"ds_read_b64_tr_b16 a[224:225], v96 offset:0\n"
"ds_read_b64_tr_b16 a[226:227], v96 offset:1536\n"
"ds_read_b64_tr_b16 a[232:233], v97 offset:0\n"
"ds_read_b64_tr_b16 a[234:235], v97 offset:1536\n"
"s_waitcnt vmcnt(18)\n"
"s_waitcnt lgkmcnt(0)\n"
"s_barrier\n"
"s_mul_i32 s27, s22, 6144\n"
"s_add_u32 s27, s27, 114688\n"
"s_mov_b32 m0, s27\n"
"s_add_u32 s27, s27, 1024\n"
"global_load_lds_dwordx4 v86, s[58:59]\n"
"s_mov_b32 m0, s27\n"
"s_add_u32 s27, s27, 1024\n"
"global_load_lds_dwordx4 v87, s[58:59]\n"
"s_mov_b32 m0, s27\n"
"s_add_u32 s27, s27, 1024\n"
"global_load_lds_dwordx4 v88, s[58:59]\n"
"s_mov_b32 m0, s27\n"
"s_add_u32 s27, s27, 1024\n"
"global_load_lds_dwordx4 v89, s[58:59]\n"
"s_mov_b32 m0, s27\n"
"s_add_u32 s27, s27, 1024\n"
"global_load_lds_dwordx4 v90, s[58:59]\n"
"s_mov_b32 m0, s27\n"
"s_nop 0\n"
"global_load_lds_dwordx4 v91, s[58:59]\n"
"s_add_u32 s58, s58, 0x60000\n"
"s_addc_u32 s59, s59, 0\n"
"ds_read_b64_tr_b16 a[196:197], v92 offset:24576\n"
"ds_read_b64_tr_b16 a[198:199], v92 offset:26112\n"
"ds_read_b64_tr_b16 a[204:205], v93 offset:24576\n"
"ds_read_b64_tr_b16 a[206:207], v93 offset:26112\n"
"ds_read_b64_tr_b16 a[212:213], v94 offset:24576\n"
"ds_read_b64_tr_b16 a[214:215], v94 offset:26112\n"
"ds_read_b64_tr_b16 a[220:221], v95 offset:24576\n"
"ds_read_b64_tr_b16 a[222:223], v95 offset:26112\n"
"ds_read_b64_tr_b16 a[228:229], v96 offset:24576\n"
"ds_read_b64_tr_b16 a[230:231], v96 offset:26112\n"
"ds_read_b64_tr_b16 a[236:237], v97 offset:24576\n"
"ds_read_b64_tr_b16 a[238:239], v97 offset:26112\n"
"s_waitcnt vmcnt(18)\n"
"s_waitcnt lgkmcnt(0)\n"
"s_barrier\n"
"s_mul_i32 s27, s22, 6144\n"
"s_add_u32 s27, s27, 139264\n"
"s_mov_b32 m0, s27\n"
"s_add_u32 s27, s27, 1024\n"
"global_load_lds_dwordx4 v86, s[58:59]\n"
"s_mov_b32 m0, s27\n"
"s_add_u32 s27, s27, 1024\n"
"global_load_lds_dwordx4 v87, s[58:59]\n"
"s_mov_b32 m0, s27\n"
"s_add_u32 s27, s27, 1024\n"
"global_load_lds_dwordx4 v88, s[58:59]\n"
"s_mov_b32 m0, s27\n"
"s_add_u32 s27, s27, 1024\n"
"global_load_lds_dwordx4 v89, s[58:59]\n"
"s_mov_b32 m0, s27\n"
"s_add_u32 s27, s27, 1024\n"
"global_load_lds_dwordx4 v90, s[58:59]\n"
"s_mov_b32 m0, s27\n"
"s_nop 0\n"
"global_load_lds_dwordx4 v91, s[58:59]\n"
"s_add_u32 s58, s58, 0x60000\n"
"s_addc_u32 s59, s59, 0\n"
"v_subrev_u32 v92, 0x1c000, v92\n"
"v_subrev_u32 v93, 0x1c000, v93\n"
"v_subrev_u32 v94, 0x1c000, v94\n"
"v_subrev_u32 v95, 0x1c000, v95\n"
"v_subrev_u32 v96, 0x1c000, v96\n"
"v_subrev_u32 v97, 0x1c000, v97\n"
"ds_read_b64_tr_b16 a[240:241], v92 offset:0\n"
"ds_read_b64_tr_b16 a[242:243], v92 offset:1536\n"
"ds_read_b64_tr_b16 a[248:249], v93 offset:0\n"
"ds_read_b64_tr_b16 a[250:251], v93 offset:1536\n"
"ds_read_b64_tr_b16 v[104:105], v94 offset:0\n"
"ds_read_b64_tr_b16 v[106:107], v94 offset:1536\n"
"ds_read_b64_tr_b16 v[112:113], v95 offset:0\n"
"ds_read_b64_tr_b16 v[114:115], v95 offset:1536\n"
"ds_read_b64_tr_b16 v[120:121], v96 offset:0\n"
"ds_read_b64_tr_b16 v[122:123], v96 offset:1536\n"
"ds_read_b64_tr_b16 v[128:129], v97 offset:0\n"
"ds_read_b64_tr_b16 v[130:131], v97 offset:1536\n"
"s_waitcnt vmcnt(18)\n"
"s_waitcnt lgkmcnt(0)\n"
"s_barrier\n"
"global_load_dwordx2 v[10:11], v9, s[92:93] sc1\n"
"s_mul_i32 s27, s22, 6144\n"
"s_add_u32 s27, s27, 0\n"
"s_mov_b32 m0, s27\n"
"s_add_u32 s27, s27, 1024\n"
"global_load_lds_dwordx4 v86, s[58:59]\n"
"s_mov_b32 m0, s27\n"
"s_add_u32 s27, s27, 1024\n"
"global_load_lds_dwordx4 v87, s[58:59]\n"
"s_mov_b32 m0, s27\n"
"s_add_u32 s27, s27, 1024\n"
"global_load_lds_dwordx4 v88, s[58:59]\n"
"s_mov_b32 m0, s27\n"
"s_add_u32 s27, s27, 1024\n"
"global_load_lds_dwordx4 v89, s[58:59]\n"
"s_mov_b32 m0, s27\n"
"s_add_u32 s27, s27, 1024\n"
"global_load_lds_dwordx4 v90, s[58:59]\n"
"s_mov_b32 m0, s27\n"
"s_nop 0\n"
"global_load_lds_dwordx4 v91, s[58:59]\n"
"s_add_u32 s58, s58, 0x60000\n"
"s_addc_u32 s59, s59, 0\n"
"ds_read_b64_tr_b16 a[244:245], v92 offset:24576\n"
"ds_read_b64_tr_b16 a[246:247], v92 offset:26112\n"
"ds_read_b64_tr_b16 a[252:253], v93 offset:24576\n"
"ds_read_b64_tr_b16 a[254:255], v93 offset:26112\n"
"ds_read_b64_tr_b16 v[108:109], v94 offset:24576\n"
"ds_read_b64_tr_b16 v[110:111], v94 offset:26112\n"
"ds_read_b64_tr_b16 v[116:117], v95 offset:24576\n"
"ds_read_b64_tr_b16 v[118:119], v95 offset:26112\n"
"ds_read_b64_tr_b16 v[124:125], v96 offset:24576\n"
"ds_read_b64_tr_b16 v[126:127], v96 offset:26112\n"
"ds_read_b64_tr_b16 v[132:133], v97 offset:24576\n"
"ds_read_b64_tr_b16 v[134:135], v97 offset:26112\n"
"s_waitcnt vmcnt(18)\n"
"s_waitcnt lgkmcnt(0)\n"
"s_barrier\n"
"s_mul_i32 s27, s22, 6144\n"
"s_add_u32 s27, s27, 24576\n"
"s_mov_b32 m0, s27\n"
"s_add_u32 s27, s27, 1024\n"
"global_load_lds_dwordx4 v86, s[58:59]\n"
"s_mov_b32 m0, s27\n"
"s_add_u32 s27, s27, 1024\n"
"global_load_lds_dwordx4 v87, s[58:59]\n"
"s_mov_b32 m0, s27\n"
"s_add_u32 s27, s27, 1024\n"
"global_load_lds_dwordx4 v88, s[58:59]\n"
"s_mov_b32 m0, s27\n"
"s_add_u32 s27, s27, 1024\n"
"global_load_lds_dwordx4 v89, s[58:59]\n"
"s_mov_b32 m0, s27\n"
"s_add_u32 s27, s27, 1024\n"
"global_load_lds_dwordx4 v90, s[58:59]\n"
"s_mov_b32 m0, s27\n"
"s_nop 0\n"
"global_load_lds_dwordx4 v91, s[58:59]\n"
"s_add_u32 s58, s58, 0x60000\n"
"s_addc_u32 s59, s59, 0\n"
"ds_read_b64_tr_b16 v[136:137], v92 offset:49152\n"
"ds_read_b64_tr_b16 v[138:139], v92 offset:50688\n"
"ds_read_b64_tr_b16 v[144:145], v93 offset:49152\n"
"ds_read_b64_tr_b16 v[146:147], v93 offset:50688\n"
"ds_read_b64_tr_b16 v[152:153], v94 offset:49152\n"
"ds_read_b64_tr_b16 v[154:155], v94 offset:50688\n"
"ds_read_b64_tr_b16 v[160:161], v95 offset:49152\n"
"ds_read_b64_tr_b16 v[162:163], v95 offset:50688\n"
"ds_read_b64_tr_b16 v[168:169], v96 offset:49152\n"
"ds_read_b64_tr_b16 v[170:171], v96 offset:50688\n"
"ds_read_b64_tr_b16 v[176:177], v97 offset:49152\n"
"ds_read_b64_tr_b16 v[178:179], v97 offset:50688\n"
"s_waitcnt vmcnt(18)\n"
"s_waitcnt lgkmcnt(0)\n"
"s_barrier\n"
"s_mul_i32 s27, s22, 6144\n"
"s_add_u32 s27, s27, 49152\n"
"s_mov_b32 m0, s27\n"
"s_add_u32 s27, s27, 1024\n"
"global_load_lds_dwordx4 v86, s[58:59]\n"
"s_mov_b32 m0, s27\n"
"s_add_u32 s27, s27, 1024\n"
"global_load_lds_dwordx4 v87, s[58:59]\n"
"s_mov_b32 m0, s27\n"
"s_add_u32 s27, s27, 1024\n"
"global_load_lds_dwordx4 v88, s[58:59]\n"
"s_mov_b32 m0, s27\n"
"s_add_u32 s27, s27, 1024\n"
"global_load_lds_dwordx4 v89, s[58:59]\n"
"s_mov_b32 m0, s27\n"
"s_add_u32 s27, s27, 1024\n"
"global_load_lds_dwordx4 v90, s[58:59]\n"
"s_mov_b32 m0, s27\n"
"s_nop 0\n"
"global_load_lds_dwordx4 v91, s[58:59]\n"
"s_add_u32 s58, s58, 0x60000\n"
"s_addc_u32 s59, s59, 0\n"
"v_add_u32 v92, 0x1c000, v92\n"
"v_add_u32 v93, 0x1c000, v93\n"
"v_add_u32 v94, 0x1c000, v94\n"
"v_add_u32 v95, 0x1c000, v95\n"
"v_add_u32 v96, 0x1c000, v96\n"
"v_add_u32 v97, 0x1c000, v97\n"
"ds_read_b64_tr_b16 v[140:141], v92 offset:0\n"
"ds_read_b64_tr_b16 v[142:143], v92 offset:1536\n"
"ds_read_b64_tr_b16 v[148:149], v93 offset:0\n"
"ds_read_b64_tr_b16 v[150:151], v93 offset:1536\n"
"ds_read_b64_tr_b16 v[156:157], v94 offset:0\n"
"ds_read_b64_tr_b16 v[158:159], v94 offset:1536\n"
"ds_read_b64_tr_b16 v[164:165], v95 offset:0\n"
"ds_read_b64_tr_b16 v[166:167], v95 offset:1536\n"
"ds_read_b64_tr_b16 v[172:173], v96 offset:0\n"
"ds_read_b64_tr_b16 v[174:175], v96 offset:1536\n"
"ds_read_b64_tr_b16 v[180:181], v97 offset:0\n"
"ds_read_b64_tr_b16 v[182:183], v97 offset:1536\n"
"s_waitcnt vmcnt(18)\n"
"s_waitcnt lgkmcnt(0)\n"
"s_barrier\n"
"s_mul_i32 s27, s22, 6144\n"
"s_add_u32 s27, s27, 114688\n"
"s_mov_b32 m0, s27\n"
"s_add_u32 s27, s27, 1024\n"
"global_load_lds_dwordx4 v86, s[58:59]\n"
"s_mov_b32 m0, s27\n"
"s_add_u32 s27, s27, 1024\n"
"global_load_lds_dwordx4 v87, s[58:59]\n"
"s_mov_b32 m0, s27\n"
"s_add_u32 s27, s27, 1024\n"
"global_load_lds_dwordx4 v88, s[58:59]\n"
"s_mov_b32 m0, s27\n"
"s_add_u32 s27, s27, 1024\n"
"global_load_lds_dwordx4 v89, s[58:59]\n"
"s_mov_b32 m0, s27\n"
"s_add_u32 s27, s27, 1024\n"
"global_load_lds_dwordx4 v90, s[58:59]\n"
"s_mov_b32 m0, s27\n"
"s_nop 0\n"
"global_load_lds_dwordx4 v91, s[58:59]\n"
"s_add_u32 s58, s58, 0x60000\n"
"s_addc_u32 s59, s59, 0\n"
"ds_read_b64_tr_b16 v[184:185], v92 offset:24576\n"
"ds_read_b64_tr_b16 v[186:187], v92 offset:26112\n"
"ds_read_b64_tr_b16 v[192:193], v93 offset:24576\n"
"ds_read_b64_tr_b16 v[194:195], v93 offset:26112\n"
"ds_read_b64_tr_b16 v[200:201], v94 offset:24576\n"
"ds_read_b64_tr_b16 v[202:203], v94 offset:26112\n"
"ds_read_b64_tr_b16 v[208:209], v95 offset:24576\n"
"ds_read_b64_tr_b16 v[210:211], v95 offset:26112\n"
"ds_read_b64_tr_b16 v[216:217], v96 offset:24576\n"
"ds_read_b64_tr_b16 v[218:219], v96 offset:26112\n"
"ds_read_b64_tr_b16 v[224:225], v97 offset:24576\n"
"ds_read_b64_tr_b16 v[226:227], v97 offset:26112\n"
"s_waitcnt vmcnt(18)\n"
"s_waitcnt lgkmcnt(0)\n"
"s_barrier\n"
"s_mul_i32 s27, s22, 6144\n"
"s_add_u32 s27, s27, 139264\n"
"s_mov_b32 m0, s27\n"
"s_add_u32 s27, s27, 1024\n"
"global_load_lds_dwordx4 v86, s[58:59]\n"
"s_mov_b32 m0, s27\n"
"s_add_u32 s27, s27, 1024\n"
"global_load_lds_dwordx4 v87, s[58:59]\n"
"s_mov_b32 m0, s27\n"
"s_add_u32 s27, s27, 1024\n"
"global_load_lds_dwordx4 v88, s[58:59]\n"
"s_mov_b32 m0, s27\n"
"s_add_u32 s27, s27, 1024\n"
"global_load_lds_dwordx4 v89, s[58:59]\n"
"s_mov_b32 m0, s27\n"
"s_add_u32 s27, s27, 1024\n"
"global_load_lds_dwordx4 v90, s[58:59]\n"
"s_mov_b32 m0, s27\n"
"s_nop 0\n"
"global_load_lds_dwordx4 v91, s[58:59]\n"
"s_add_u32 s58, s58, 0x60000\n"
"s_addc_u32 s59, s59, 0\n"
".La_hs_1:\n"
"v_cmp_ne_u32 vcc, v84, v10\n"
"s_cbranch_vccz .La_hso_2\n"
"global_load_dwordx2 v[10:11], v9, s[92:93] sc1\n"
"s_waitcnt vmcnt(0)\n"
"s_add_u32 s26, s26, 1\n"
"s_cmp_lt_u32 s26, 0x80000\n"
"s_cbranch_scc1 .La_hs_1\n"
"s_branch .La_abort\n"
".La_hso_2:\n"
"v_readfirstlane_b32 s68, v11\n"
"v_xor_b32 v84, 0x80000000, v84\n"
"s_mov_b64 exec, 1\n"
"global_store_dword v9, v84, s[90:91] sc1\n"
"s_mov_b64 exec, -1\n"
"s_mov_b32 s70, 0\n"
"s_lshr_b32 s27, s68, 0\n"
"s_xor_b32 s27, s27, s24\n"
"s_and_b32 s27, s27, 1\n"
"s_lshl_b32 s27, s27, 13\n"
"s_add_u32 s27, s27, s70\n"
"s_add_u32 s56, s20, s27\n"
"s_addc_u32 s57, s21, 0\n"
"s_add_u32 s27, s70, 0x618000\n"
"s_add_u32 s30, s20, s27\n"
"s_addc_u32 s31, s21, 0\n"
"s_cmp_eq_u32 s24, 0\n"
"s_cselect_b64 s[30:31], s[30:31], s[56:57]\n"
"s_lshr_b32 s27, s68, 0\n"
"s_xor_b32 s27, s27, s24\n"
"s_and_b32 s27, s27, 1\n"
"s_lshl_b32 s27, s27, 13\n"
"s_add_u32 s27, s27, 0x180000\n"
"s_mul_i32 s28, s23, 8192\n"
"s_lshr_b32 s28, s28, 16\n"
"s_lshl_b32 s28, s28, 8\n"
"s_add_u32 s27, s27, s28\n"
"s_add_u32 s98, s20, s27\n"
"s_addc_u32 s99, s21, 0\n"
"s_mul_i32 s27, s24, 40960\n"
"s_mul_i32 s28, s22, 10240\n"
"s_add_u32 s27, s27, s28\n"
"s_add_u32 s27, s27, 0x800000\n"
"s_add_u32 s96, s20, s27\n"
"s_addc_u32 s97, s21, 0\n"
"s_mul_i32 s27, s22, 10240\n"
"s_add_u32 s27, s27, 73728\n"
"s_mov_b32 m0, s27\n"
"s_add_u32 s28, s96, 0x0\n"
"s_addc_u32 s29, s97, 0\n"
"global_load_lds_dwordx4 v0, s[28:29] offset:0 sc1\n"
"global_load_lds_dwordx4 v0, s[28:29] offset:1024 sc1\n"
"global_load_lds_dwordx4 v0, s[28:29] offset:2048 sc1\n"
"global_load_lds_dwordx4 v0, s[28:29] offset:3072 sc1\n"
"s_mul_i32 s27, s22, 10240\n"
"s_add_u32 s27, s27, 77824\n"
"s_mov_b32 m0, s27\n"
"s_add_u32 s28, s96, 0x1000\n"
"s_addc_u32 s29, s97, 0\n"
"global_load_lds_dwordx4 v0, s[28:29] offset:0 sc1\n"
"global_load_lds_dwordx4 v0, s[28:29] offset:1024 sc1\n"
"global_load_lds_dwordx4 v0, s[28:29] offset:2048 sc1\n"
"global_load_lds_dwordx4 v0, s[28:29] offset:3072 sc1\n"
"s_mul_i32 s27, s22, 10240\n"
"s_add_u32 s27, s27, 81920\n"
"s_mov_b32 m0, s27\n"
"s_add_u32 s28, s96, 0x2000\n"
"s_addc_u32 s29, s97, 0\n"
"global_load_lds_dwordx4 v0, s[28:29] offset:0 sc1\n"
"global_load_lds_dwordx4 v0, s[28:29] offset:1024 sc1\n"
"s_add_u32 s96, s96, 0x14000\n"
"s_addc_u32 s97, s97, 0\n"
"s_lshr_b32 s27, s68, 1\n"
"s_xor_b32 s27, s27, s24\n"
"s_and_b32 s27, s27, 1\n"
"s_lshl_b32 s27, s27, 13\n"
"s_lshr_b32 s28, s23, 3\n"
"s_lshl_b32 s28, s28, 14\n"
"s_add_u32 s27, s27, s28\n"
"s_and_b32 s28, s23, 7\n"
"s_lshl_b32 s28, s28, 10\n"
"s_add_u32 s27, s27, s28\n"
"s_add_u32 s27, s27, 0x200000\n"
"s_add_u32 s34, s20, s27\n"
"s_addc_u32 s35, s21, 0\n"
"s_mul_i32 s27, s24, 81920\n"
"s_mul_i32 s28, s22, 40960\n"
"s_add_u32 s27, s27, s28\n"
"s_lshr_b32 s28, s23, 3\n"
"s_mul_i32 s28, s28, 10240\n"
"s_add_u32 s27, s27, s28\n"
"s_and_b32 s28, s23, 7\n"
"s_lshl_b32 s28, s28, 10\n"
"s_add_u32 s27, s27, s28\n"
"s_add_u32 s27, s27, 0x13f6000\n"
"s_add_u32 s36, s20, s27\n"
"s_addc_u32 s37, s21, 0\n"
"s_lshl_b32 s27, s24, 1\n"
"s_add_u32 s27, s27, s22\n"
"s_lshl_b32 s27, s27, 5\n"
"s_add_u32 s27, s27, s23\n"
"s_lshl_b32 s27, s27, 7\n"
"s_add_u32 s27, s27, 0x607000\n"
"s_add_u32 s38, s20, s27\n"
"s_addc_u32 s39, s21, 0\n"
"v_subrev_u32 v92, 0x1c000, v92\n"
"v_subrev_u32 v93, 0x1c000, v93\n"
"v_subrev_u32 v94, 0x1c000, v94\n"
"v_subrev_u32 v95, 0x1c000, v95\n"
"v_subrev_u32 v96, 0x1c000, v96\n"
"v_subrev_u32 v97, 0x1c000, v97\n"
"ds_read_b64_tr_b16 v[188:189], v92 offset:0\n"
"ds_read_b64_tr_b16 v[190:191], v92 offset:1536\n"
"ds_read_b64_tr_b16 v[196:197], v93 offset:0\n"
"ds_read_b64_tr_b16 v[198:199], v93 offset:1536\n"
"ds_read_b64_tr_b16 v[204:205], v94 offset:0\n"
"ds_read_b64_tr_b16 v[206:207], v94 offset:1536\n"
"ds_read_b64_tr_b16 v[212:213], v95 offset:0\n"
"ds_read_b64_tr_b16 v[214:215], v95 offset:1536\n"
"ds_read_b64_tr_b16 v[220:221], v96 offset:0\n"
"ds_read_b64_tr_b16 v[222:223], v96 offset:1536\n"
"ds_read_b64_tr_b16 v[228:229], v97 offset:0\n"
"ds_read_b64_tr_b16 v[230:231], v97 offset:1536\n"
"s_waitcnt vmcnt(29)\n"
"s_waitcnt lgkmcnt(0)\n"
"s_barrier\n"
"ds_read_b64_tr_b16 v[12:13], v92 offset:24576\n"
"ds_read_b64_tr_b16 v[14:15], v92 offset:26112\n"
"ds_read_b64_tr_b16 v[20:21], v93 offset:24576\n"
"ds_read_b64_tr_b16 v[22:23], v93 offset:26112\n"
"ds_read_b64_tr_b16 v[28:29], v94 offset:24576\n"
"ds_read_b64_tr_b16 v[30:31], v94 offset:26112\n"
"ds_read_b64_tr_b16 v[232:233], v95 offset:24576\n"
"ds_read_b64_tr_b16 v[234:235], v95 offset:26112\n"
"ds_read_b64_tr_b16 v[240:241], v96 offset:24576\n"
"ds_read_b64_tr_b16 v[242:243], v96 offset:26112\n"
"ds_read_b64_tr_b16 v[248:249], v97 offset:24576\n"
"ds_read_b64_tr_b16 v[250:251], v97 offset:26112\n"
"s_waitcnt vmcnt(23)\n"
"s_waitcnt lgkmcnt(0)\n"
"s_barrier\n"
"ds_read_b64_tr_b16 v[16:17], v92 offset:49152\n"
"ds_read_b64_tr_b16 v[18:19], v92 offset:50688\n"
"ds_read_b64_tr_b16 v[24:25], v93 offset:49152\n"
"ds_read_b64_tr_b16 v[26:27], v93 offset:50688\n"
"ds_read_b64_tr_b16 v[32:33], v94 offset:49152\n"
"ds_read_b64_tr_b16 v[34:35], v94 offset:50688\n"
"ds_read_b64_tr_b16 v[236:237], v95 offset:49152\n"
"ds_read_b64_tr_b16 v[238:239], v95 offset:50688\n"
"ds_read_b64_tr_b16 v[244:245], v96 offset:49152\n"
"ds_read_b64_tr_b16 v[246:247], v96 offset:50688\n"
"ds_read_b64_tr_b16 v[252:253], v97 offset:49152\n"
"ds_read_b64_tr_b16 v[254:255], v97 offset:50688\n"
"s_waitcnt vmcnt(17)\n"
"s_waitcnt lgkmcnt(0)\n"
"s_barrier\n"
"v_add_u32 v92, 0x1c000, v92\n"
"v_add_u32 v93, 0x1c000, v93\n"
"v_add_u32 v94, 0x1c000, v94\n"
"v_add_u32 v95, 0x1c000, v95\n"
"v_add_u32 v96, 0x1c000, v96\n"
"v_add_u32 v97, 0x1c000, v97\n"
"ds_read_b64_tr_b16 v[36:37], v92 offset:0\n"
"ds_read_b64_tr_b16 v[38:39], v92 offset:1536\n"
"ds_read_b64_tr_b16 v[44:45], v93 offset:0\n"
"ds_read_b64_tr_b16 v[46:47], v93 offset:1536\n"
"ds_read_b64_tr_b16 v[52:53], v94 offset:0\n"
"ds_read_b64_tr_b16 v[54:55], v94 offset:1536\n"
"ds_read_b64_tr_b16 v[60:61], v95 offset:0\n"
"ds_read_b64_tr_b16 v[62:63], v95 offset:1536\n"
"ds_read_b64_tr_b16 v[68:69], v96 offset:0\n"
"ds_read_b64_tr_b16 v[70:71], v96 offset:1536\n"
"ds_read_b64_tr_b16 v[76:77], v97 offset:0\n"
"ds_read_b64_tr_b16 v[78:79], v97 offset:1536\n"
"s_waitcnt vmcnt(11)\n"
"s_waitcnt lgkmcnt(0)\n"
"s_barrier\n"
"ds_read_b64_tr_b16 v[40:41], v92 offset:24576\n"
"ds_read_b64_tr_b16 v[42:43], v92 offset:26112\n"
"ds_read_b64_tr_b16 v[48:49], v93 offset:24576\n"
"ds_read_b64_tr_b16 v[50:51], v93 offset:26112\n"
"ds_read_b64_tr_b16 v[56:57], v94 offset:24576\n"
"ds_read_b64_tr_b16 v[58:59], v94 offset:26112\n"
"ds_read_b64_tr_b16 v[64:65], v95 offset:24576\n"
"ds_read_b64_tr_b16 v[66:67], v95 offset:26112\n"
"ds_read_b64_tr_b16 v[72:73], v96 offset:24576\n"
"ds_read_b64_tr_b16 v[74:75], v96 offset:26112\n"
"ds_read_b64_tr_b16 v[80:81], v97 offset:24576\n"
"ds_read_b64_tr_b16 v[82:83], v97 offset:26112\n"
"s_waitcnt vmcnt(0)\n"
"s_waitcnt lgkmcnt(0)\n"
"s_barrier\n"
"ds_write_b128 v4, v[12:15] offset:0\n"
"ds_write_b128 v4, v[16:19] offset:1024\n"
"ds_write_b128 v4, v[20:23] offset:2048\n"
"ds_write_b128 v4, v[24:27] offset:3072\n"
"ds_write_b128 v4, v[28:31] offset:4096\n"
"ds_write_b128 v4, v[32:35] offset:5120\n"
"ds_write_b128 v4, v[36:39] offset:6144\n"
"ds_write_b128 v4, v[40:43] offset:7168\n"
"ds_write_b128 v4, v[44:47] offset:8192\n"
"ds_write_b128 v4, v[48:51] offset:9216\n"
"ds_write_b128 v4, v[52:55] offset:10240\n"
"ds_write_b128 v4, v[56:59] offset:11264\n"
"ds_write_b128 v4, v[60:63] offset:12288\n"
"ds_write_b128 v4, v[64:67] offset:13312\n"
"ds_write_b128 v4, v[68:71] offset:14336\n"
"ds_write_b128 v4, v[72:75] offset:15360\n"
"ds_write_b128 v4, v[76:79] offset:16384\n"
"ds_write_b128 v4, v[80:83] offset:17408\n"
"s_waitcnt lgkmcnt(0)\n"
"s_barrier\n"
"s_lshr_b32 s27, s68, 0\n"
"s_xor_b32 s27, s27, s24\n"
"s_and_b32 s27, s27, 1\n"
"s_lshl_b32 s27, s27, 13\n"
"s_add_u32 s27, s27, 0x184000\n"
"s_add_u32 s58, s20, s27\n"
"s_addc_u32 s59, s21, 0\n"
"s_mov_b32 s46, 0xf\n"
"s_mov_b32 s47, 0x0\n"
"s_cmp_eq_u32 s23, 0\n"
"s_cselect_b64 s[46:47], s[46:47], 0\n"
".La_top_3:\n"
"s_nop 7\n"
"ds_read_b128 v[58:61], v5 offset:0\n"
"ds_read_b128 v[62:65], v5 offset:1024\n"
"s_cmp_lg_u32 s22, 3\n"
"s_cbranch_scc1 .La_ncl_4\n"
"s_cmp_ge_u32 s25, 60\n"
"s_cbranch_scc1 .La_ncl_4\n"
"v_mov_b64 v[98:99], -1\n"
"v_mov_b64 v[100:101], -1\n"
"s_add_u32 s28, s34, 0x20000\n"
"s_addc_u32 s29, s35, 0\n"
"s_mov_b32 exec_hi, 0xffff\n"
"global_store_dwordx4 v0, v[98:101], s[28:29] sc1\n"
"s_mov_b64 exec, -1\n"
".La_ncl_4:\n"
"s_waitcnt lgkmcnt(1)\n"
"v_mfma_f32_16x16x32_bf16 v[10:13], a[0:3], v[58:61], 0\n"
"ds_read_b128 v[66:69], v5 offset:32768\n"
"v_mfma_f32_16x16x32_bf16 v[14:17], a[8:11], v[58:61], 0\n"
"ds_read_b128 v[70:73], v5 offset:33792\n"
"v_mfma_f32_16x16x32_bf16 v[18:21], a[16:19], v[58:61], 0\n"
"ds_read_b128 v[74:77], v4 offset:0\n"
"v_mfma_f32_16x16x32_bf16 v[22:25], a[24:27], v[58:61], 0\n"
"ds_read_b128 v[78:81], v4 offset:2048\n"
"v_mfma_f32_16x16x32_bf16 v[26:29], a[32:35], v[58:61], 0\n"
"ds_read_b128 v[82:85], v4 offset:4096\n"
"v_mfma_f32_16x16x32_bf16 v[30:33], a[40:43], v[58:61], 0\n"
"s_waitcnt lgkmcnt(5)\n"
"v_mfma_f32_16x16x32_bf16 v[34:37], a[0:3], v[62:65], 0\n"
"v_mfma_f32_16x16x32_bf16 v[38:41], a[8:11], v[62:65], 0\n"
"v_mfma_f32_16x16x32_bf16 v[42:45], a[16:19], v[62:65], 0\n"
"v_mfma_f32_16x16x32_bf16 v[46:49], a[24:27], v[62:65], 0\n"
"v_mfma_f32_16x16x32_bf16 v[50:53], a[32:35], v[62:65], 0\n"
"v_mfma_f32_16x16x32_bf16 v[54:57], a[40:43], v[62:65], 0\n"
"s_waitcnt lgkmcnt(2)\n"
"v_mfma_f32_16x16x32_bf16 v[10:13], v[74:77], v[66:69], v[10:13]\n"
"ds_read_b128 v[58:61], v5 offset:2048\n"
"s_waitcnt lgkmcnt(2)\n"
"v_mfma_f32_16x16x32_bf16 v[14:17], v[78:81], v[66:69], v[14:17]\n"
"ds_read_b128 v[62:65], v5 offset:3072\n"
"s_waitcnt lgkmcnt(2)\n"
"v_mfma_f32_16x16x32_bf16 v[18:21], v[82:85], v[66:69], v[18:21]\n"
"v_mfma_f32_16x16x32_bf16 v[22:25], v[232:235], v[66:69], v[22:25]\n"
"v_mfma_f32_16x16x32_bf16 v[26:29], v[240:243], v[66:69], v[26:29]\n"
"v_mfma_f32_16x16x32_bf16 v[30:33], v[248:251], v[66:69], v[30:33]\n"
"v_mfma_f32_16x16x32_bf16 v[34:37], v[74:77], v[70:73], v[34:37]\n"
"v_mfma_f32_16x16x32_bf16 v[38:41], v[78:81], v[70:73], v[38:41]\n"
"v_mfma_f32_16x16x32_bf16 v[42:45], v[82:85], v[70:73], v[42:45]\n"
"v_mfma_f32_16x16x32_bf16 v[46:49], v[232:235], v[70:73], v[46:49]\n"
"v_mfma_f32_16x16x32_bf16 v[50:53], v[240:243], v[70:73], v[50:53]\n"
"v_mfma_f32_16x16x32_bf16 v[54:57], v[248:251], v[70:73], v[54:57]\n"
"s_waitcnt lgkmcnt(1)\n"
"v_mfma_f32_16x16x32_bf16 v[10:13], a[4:7], v[58:61], v[10:13]\n"
"ds_read_b128 v[66:69], v5 offset:34816\n"
"v_mfma_f32_16x16x32_bf16 v[14:17], a[12:15], v[58:61], v[14:17]\n"
"ds_read_b128 v[70:73], v5 offset:35840\n"
"v_mfma_f32_16x16x32_bf16 v[18:21], a[20:23], v[58:61], v[18:21]\n"
"ds_read_b128 v[74:77], v4 offset:1024\n"
"v_mfma_f32_16x16x32_bf16 v[22:25], a[28:31], v[58:61], v[22:25]\n"
"ds_read_b128 v[78:81], v4 offset:3072\n"
"v_mfma_f32_16x16x32_bf16 v[26:29], a[36:39], v[58:61], v[26:29]\n"
"ds_read_b128 v[82:85], v4 offset:5120\n"
"v_mfma_f32_16x16x32_bf16 v[30:33], a[44:47], v[58:61], v[30:33]\n"
"s_waitcnt lgkmcnt(5)\n"
"v_mfma_f32_16x16x32_bf16 v[34:37], a[4:7], v[62:65], v[34:37]\n"
"v_mfma_f32_16x16x32_bf16 v[38:41], a[12:15], v[62:65], v[38:41]\n"
"v_mfma_f32_16x16x32_bf16 v[42:45], a[20:23], v[62:65], v[42:45]\n"
"v_mfma_f32_16x16x32_bf16 v[46:49], a[28:31], v[62:65], v[46:49]\n"
"v_mfma_f32_16x16x32_bf16 v[50:53], a[36:39], v[62:65], v[50:53]\n"
"v_mfma_f32_16x16x32_bf16 v[54:57], a[44:47], v[62:65], v[54:57]\n"
"s_waitcnt lgkmcnt(2)\n"
"v_mfma_f32_16x16x32_bf16 v[10:13], v[74:77], v[66:69], v[10:13]\n"
"ds_read_b128 v[58:61], v5 offset:4096\n"
"s_waitcnt lgkmcnt(2)\n"
"v_mfma_f32_16x16x32_bf16 v[14:17], v[78:81], v[66:69], v[14:17]\n"
"ds_read_b128 v[62:65], v5 offset:5120\n"
"s_waitcnt lgkmcnt(2)\n"
"v_mfma_f32_16x16x32_bf16 v[18:21], v[82:85], v[66:69], v[18:21]\n"
"v_mfma_f32_16x16x32_bf16 v[22:25], v[236:239], v[66:69], v[22:25]\n"
"v_mfma_f32_16x16x32_bf16 v[26:29], v[244:247], v[66:69], v[26:29]\n"
"v_mfma_f32_16x16x32_bf16 v[30:33], v[252:255], v[66:69], v[30:33]\n"
"v_mfma_f32_16x16x32_bf16 v[34:37], v[74:77], v[70:73], v[34:37]\n"
"v_mfma_f32_16x16x32_bf16 v[38:41], v[78:81], v[70:73], v[38:41]\n"
"v_mfma_f32_16x16x32_bf16 v[42:45], v[82:85], v[70:73], v[42:45]\n"
"v_mfma_f32_16x16x32_bf16 v[46:49], v[236:239], v[70:73], v[46:49]\n"
"v_mfma_f32_16x16x32_bf16 v[50:53], v[244:247], v[70:73], v[50:53]\n"
"v_mfma_f32_16x16x32_bf16 v[54:57], v[252:255], v[70:73], v[54:57]\n"
"s_waitcnt lgkmcnt(1)\n"
"v_mfma_f32_16x16x32_bf16 v[10:13], a[48:51], v[58:61], v[10:13]\n"
"ds_read_b128 v[66:69], v5 offset:36864\n"
"v_mfma_f32_16x16x32_bf16 v[14:17], a[56:59], v[58:61], v[14:17]\n"
"ds_read_b128 v[70:73], v5 offset:37888\n"
"v_mfma_f32_16x16x32_bf16 v[18:21], a[64:67], v[58:61], v[18:21]\n"
"ds_read_b128 v[74:77], v4 offset:6144\n"
"v_mfma_f32_16x16x32_bf16 v[22:25], a[72:75], v[58:61], v[22:25]\n"
"ds_read_b128 v[78:81], v4 offset:8192\n"
"v_mfma_f32_16x16x32_bf16 v[26:29], a[80:83], v[58:61], v[26:29]\n"
"ds_read_b128 v[82:85], v4 offset:10240\n"
"v_mfma_f32_16x16x32_bf16 v[30:33], a[88:91], v[58:61], v[30:33]\n"
"ds_read_b128 v[86:89], v4 offset:12288\n"
"s_waitcnt lgkmcnt(6)\n"
"v_mfma_f32_16x16x32_bf16 v[34:37], a[48:51], v[62:65], v[34:37]\n"
"ds_read_b128 v[90:93], v4 offset:14336\n"
"v_mfma_f32_16x16x32_bf16 v[38:41], a[56:59], v[62:65], v[38:41]\n"
"ds_read_b128 v[94:97], v4 offset:16384\n"
"v_mfma_f32_16x16x32_bf16 v[42:45], a[64:67], v[62:65], v[42:45]\n"
"v_mfma_f32_16x16x32_bf16 v[46:49], a[72:75], v[62:65], v[46:49]\n"
"v_mfma_f32_16x16x32_bf16 v[50:53], a[80:83], v[62:65], v[50:53]\n"
"v_mfma_f32_16x16x32_bf16 v[54:57], a[88:91], v[62:65], v[54:57]\n"
"s_sub_u32 s27, s22, 1\n"
"s_cmp_ge_u32 s27, 2\n"
"s_cbranch_scc1 .La_nofl_5\n"
"s_cmp_lt_u32 s25, 2\n"
"s_cbranch_scc1 .La_nofl_5\n"
"s_waitcnt vmcnt(0)\n"
"s_sub_u32 s25, s25, 2\n"
"s_mov_b64 exec, 1\n"
"s_add_u32 s27, s25, 1\n"
"v_mov_b32 v100, s27\n"
"v_mov_b32 v101, 0\n"
"global_store_dword v101, v100, s[38:39] sc1\n"
"s_mov_b64 exec, -1\n"
"s_add_u32 s25, s25, 2\n"
".La_nofl_5:\n"
"s_waitcnt lgkmcnt(5)\n"
"v_mfma_f32_16x16x32_bf16 v[10:13], v[74:77], v[66:69], v[10:13]\n"
"ds_read_b128 v[58:61], v5 offset:6144\n"
"s_waitcnt lgkmcnt(5)\n"
"v_mfma_f32_16x16x32_bf16 v[14:17], v[78:81], v[66:69], v[14:17]\n"
"ds_read_b128 v[62:65], v5 offset:7168\n"
"s_waitcnt lgkmcnt(5)\n"
"v_mfma_f32_16x16x32_bf16 v[18:21], v[82:85], v[66:69], v[18:21]\n"
"s_waitcnt lgkmcnt(4)\n"
"v_mfma_f32_16x16x32_bf16 v[22:25], v[86:89], v[66:69], v[22:25]\n"
"s_waitcnt lgkmcnt(3)\n"
"v_mfma_f32_16x16x32_bf16 v[26:29], v[90:93], v[66:69], v[26:29]\n"
"s_waitcnt lgkmcnt(2)\n"
"v_mfma_f32_16x16x32_bf16 v[30:33], v[94:97], v[66:69], v[30:33]\n"
"v_mfma_f32_16x16x32_bf16 v[34:37], v[74:77], v[70:73], v[34:37]\n"
"v_mfma_f32_16x16x32_bf16 v[38:41], v[78:81], v[70:73], v[38:41]\n"
"v_mfma_f32_16x16x32_bf16 v[42:45], v[82:85], v[70:73], v[42:45]\n"
"v_mfma_f32_16x16x32_bf16 v[46:49], v[86:89], v[70:73], v[46:49]\n"
"v_mfma_f32_16x16x32_bf16 v[50:53], v[90:93], v[70:73], v[50:53]\n"
"v_mfma_f32_16x16x32_bf16 v[54:57], v[94:97], v[70:73], v[54:57]\n"
"s_waitcnt lgkmcnt(1)\n"
"v_mfma_f32_16x16x32_bf16 v[10:13], a[52:55], v[58:61], v[10:13]\n"
"ds_read_b128 v[66:69], v5 offset:38912\n"
"v_mfma_f32_16x16x32_bf16 v[14:17], a[60:63], v[58:61], v[14:17]\n"
"ds_read_b128 v[70:73], v5 offset:39936\n"
"v_mfma_f32_16x16x32_bf16 v[18:21], a[68:71], v[58:61], v[18:21]\n"
"ds_read_b128 v[74:77], v4 offset:7168\n"
"v_mfma_f32_16x16x32_bf16 v[22:25], a[76:79], v[58:61], v[22:25]\n"
"ds_read_b128 v[78:81], v4 offset:9216\n"
"v_mfma_f32_16x16x32_bf16 v[26:29], a[84:87], v[58:61], v[26:29]\n"
"ds_read_b128 v[82:85], v4 offset:11264\n"
"v_mfma_f32_16x16x32_bf16 v[30:33], a[92:95], v[58:61], v[30:33]\n"
"ds_read_b128 v[86:89], v4 offset:13312\n"
"s_waitcnt lgkmcnt(6)\n"
"v_mfma_f32_16x16x32_bf16 v[34:37], a[52:55], v[62:65], v[34:37]\n"
"ds_read_b128 v[90:93], v4 offset:15360\n"
"v_mfma_f32_16x16x32_bf16 v[38:41], a[60:63], v[62:65], v[38:41]\n"
"ds_read_b128 v[94:97], v4 offset:17408\n"
"v_mfma_f32_16x16x32_bf16 v[42:45], a[68:71], v[62:65], v[42:45]\n"
"v_mfma_f32_16x16x32_bf16 v[46:49], a[76:79], v[62:65], v[46:49]\n"
"v_mfma_f32_16x16x32_bf16 v[50:53], a[84:87], v[62:65], v[50:53]\n"
"v_mfma_f32_16x16x32_bf16 v[54:57], a[92:95], v[62:65], v[54:57]\n"
"s_waitcnt lgkmcnt(5)\n"
"v_mfma_f32_16x16x32_bf16 v[10:13], v[74:77], v[66:69], v[10:13]\n"
"ds_read_b128 v[58:61], v5 offset:8192\n"
"s_waitcnt lgkmcnt(5)\n"
"v_mfma_f32_16x16x32_bf16 v[14:17], v[78:81], v[66:69], v[14:17]\n"
"ds_read_b128 v[62:65], v5 offset:9216\n"
"s_waitcnt lgkmcnt(5)\n"
"v_mfma_f32_16x16x32_bf16 v[18:21], v[82:85], v[66:69], v[18:21]\n"
"s_waitcnt lgkmcnt(4)\n"
"v_mfma_f32_16x16x32_bf16 v[22:25], v[86:89], v[66:69], v[22:25]\n"
"s_waitcnt lgkmcnt(3)\n"
"v_mfma_f32_16x16x32_bf16 v[26:29], v[90:93], v[66:69], v[26:29]\n"
"s_waitcnt lgkmcnt(2)\n"
"v_mfma_f32_16x16x32_bf16 v[30:33], v[94:97], v[66:69], v[30:33]\n"
"v_mfma_f32_16x16x32_bf16 v[34:37], v[74:77], v[70:73], v[34:37]\n"
"v_mfma_f32_16x16x32_bf16 v[38:41], v[78:81], v[70:73], v[38:41]\n"
"v_mfma_f32_16x16x32_bf16 v[42:45], v[82:85], v[70:73], v[42:45]\n"
"v_mfma_f32_16x16x32_bf16 v[46:49], v[86:89], v[70:73], v[46:49]\n"
"v_mfma_f32_16x16x32_bf16 v[50:53], v[90:93], v[70:73], v[50:53]\n"
"v_mfma_f32_16x16x32_bf16 v[54:57], v[94:97], v[70:73], v[54:57]\n"
"s_waitcnt lgkmcnt(1)\n"
"v_mfma_f32_16x16x32_bf16 v[10:13], a[96:99], v[58:61], v[10:13]\n"
"ds_read_b128 v[66:69], v5 offset:10240\n"
"v_mfma_f32_16x16x32_bf16 v[14:17], a[104:107], v[58:61], v[14:17]\n"
"ds_read_b128 v[70:73], v5 offset:11264\n"
"v_mfma_f32_16x16x32_bf16 v[18:21], a[112:115], v[58:61], v[18:21]\n"
"v_mfma_f32_16x16x32_bf16 v[22:25], a[120:123], v[58:61], v[22:25]\n"
"v_mfma_f32_16x16x32_bf16 v[26:29], a[128:131], v[58:61], v[26:29]\n"
"v_mfma_f32_16x16x32_bf16 v[30:33], a[136:139], v[58:61], v[30:33]\n"
"s_waitcnt lgkmcnt(2)\n"
"v_mfma_f32_16x16x32_bf16 v[34:37], a[96:99], v[62:65], v[34:37]\n"
"v_mfma_f32_16x16x32_bf16 v[38:41], a[104:107], v[62:65], v[38:41]\n"
"v_mfma_f32_16x16x32_bf16 v[42:45], a[112:115], v[62:65], v[42:45]\n"
"v_mfma_f32_16x16x32_bf16 v[46:49], a[120:123], v[62:65], v[46:49]\n"
"v_mfma_f32_16x16x32_bf16 v[50:53], a[128:131], v[62:65], v[50:53]\n"
"v_mfma_f32_16x16x32_bf16 v[54:57], a[136:139], v[62:65], v[54:57]\n"
"s_waitcnt lgkmcnt(1)\n"
"v_mfma_f32_16x16x32_bf16 v[10:13], a[100:103], v[66:69], v[10:13]\n"
"ds_read_b128 v[58:61], v5 offset:12288\n"
"v_mfma_f32_16x16x32_bf16 v[14:17], a[108:111], v[66:69], v[14:17]\n"
"ds_read_b128 v[62:65], v5 offset:13312\n"
"v_mfma_f32_16x16x32_bf16 v[18:21], a[116:119], v[66:69], v[18:21]\n"
"v_mfma_f32_16x16x32_bf16 v[22:25], a[124:127], v[66:69], v[22:25]\n"
"v_mfma_f32_16x16x32_bf16 v[26:29], a[132:135], v[66:69], v[26:29]\n"
"v_mfma_f32_16x16x32_bf16 v[30:33], a[140:143], v[66:69], v[30:33]\n"
"s_waitcnt lgkmcnt(2)\n"
"v_mfma_f32_16x16x32_bf16 v[34:37], a[100:103], v[70:73], v[34:37]\n"
"v_mfma_f32_16x16x32_bf16 v[38:41], a[108:111], v[70:73], v[38:41]\n"
"v_mfma_f32_16x16x32_bf16 v[42:45], a[116:119], v[70:73], v[42:45]\n"
"v_mfma_f32_16x16x32_bf16 v[46:49], a[124:127], v[70:73], v[46:49]\n"
"v_mfma_f32_16x16x32_bf16 v[50:53], a[132:135], v[70:73], v[50:53]\n"
"v_mfma_f32_16x16x32_bf16 v[54:57], a[140:143], v[70:73], v[54:57]\n"
"s_waitcnt lgkmcnt(1)\n"
"v_mfma_f32_16x16x32_bf16 v[10:13], a[144:147], v[58:61], v[10:13]\n"
"ds_read_b128 v[66:69], v5 offset:14336\n"
"v_mfma_f32_16x16x32_bf16 v[14:17], a[152:155], v[58:61], v[14:17]\n"
"ds_read_b128 v[70:73], v5 offset:15360\n"
"v_mfma_f32_16x16x32_bf16 v[18:21], a[160:163], v[58:61], v[18:21]\n"
"v_mfma_f32_16x16x32_bf16 v[22:25], a[168:171], v[58:61], v[22:25]\n"
"v_mfma_f32_16x16x32_bf16 v[26:29], a[176:179], v[58:61], v[26:29]\n"
"v_mfma_f32_16x16x32_bf16 v[30:33], a[184:187], v[58:61], v[30:33]\n"
"s_waitcnt lgkmcnt(2)\n"
"v_mfma_f32_16x16x32_bf16 v[34:37], a[144:147], v[62:65], v[34:37]\n"
"v_mfma_f32_16x16x32_bf16 v[38:41], a[152:155], v[62:65], v[38:41]\n"
"v_mfma_f32_16x16x32_bf16 v[42:45], a[160:163], v[62:65], v[42:45]\n"
"v_mfma_f32_16x16x32_bf16 v[46:49], a[168:171], v[62:65], v[46:49]\n"
"v_mfma_f32_16x16x32_bf16 v[50:53], a[176:179], v[62:65], v[50:53]\n"
"v_mfma_f32_16x16x32_bf16 v[54:57], a[184:187], v[62:65], v[54:57]\n"
"s_waitcnt lgkmcnt(1)\n"
"v_mfma_f32_16x16x32_bf16 v[10:13], a[148:151], v[66:69], v[10:13]\n"
"ds_read_b128 v[58:61], v5 offset:16384\n"
"v_mfma_f32_16x16x32_bf16 v[14:17], a[156:159], v[66:69], v[14:17]\n"
"ds_read_b128 v[62:65], v5 offset:17408\n"
"v_mfma_f32_16x16x32_bf16 v[18:21], a[164:167], v[66:69], v[18:21]\n"
"v_mfma_f32_16x16x32_bf16 v[22:25], a[172:175], v[66:69], v[22:25]\n"
"v_mfma_f32_16x16x32_bf16 v[26:29], a[180:183], v[66:69], v[26:29]\n"
"v_mfma_f32_16x16x32_bf16 v[30:33], a[188:191], v[66:69], v[30:33]\n"
"s_waitcnt lgkmcnt(2)\n"
"v_mfma_f32_16x16x32_bf16 v[34:37], a[148:151], v[70:73], v[34:37]\n"
"v_mfma_f32_16x16x32_bf16 v[38:41], a[156:159], v[70:73], v[38:41]\n"
"v_mfma_f32_16x16x32_bf16 v[42:45], a[164:167], v[70:73], v[42:45]\n"
"v_mfma_f32_16x16x32_bf16 v[46:49], a[172:175], v[70:73], v[46:49]\n"
"v_mfma_f32_16x16x32_bf16 v[50:53], a[180:183], v[70:73], v[50:53]\n"
"v_mfma_f32_16x16x32_bf16 v[54:57], a[188:191], v[70:73], v[54:57]\n"
"s_waitcnt lgkmcnt(1)\n"
"v_mfma_f32_16x16x32_bf16 v[10:13], a[192:195], v[58:61], v[10:13]\n"
"ds_read_b128 v[66:69], v5 offset:18432\n"
"v_mfma_f32_16x16x32_bf16 v[14:17], a[200:203], v[58:61], v[14:17]\n"
"ds_read_b128 v[70:73], v5 offset:19456\n"
"v_mfma_f32_16x16x32_bf16 v[18:21], a[208:211], v[58:61], v[18:21]\n"
"v_mfma_f32_16x16x32_bf16 v[22:25], a[216:219], v[58:61], v[22:25]\n"
"v_mfma_f32_16x16x32_bf16 v[26:29], a[224:227], v[58:61], v[26:29]\n"
"v_mfma_f32_16x16x32_bf16 v[30:33], a[232:235], v[58:61], v[30:33]\n"
"s_waitcnt lgkmcnt(2)\n"
"v_mfma_f32_16x16x32_bf16 v[34:37], a[192:195], v[62:65], v[34:37]\n"
"v_mfma_f32_16x16x32_bf16 v[38:41], a[200:203], v[62:65], v[38:41]\n"
"v_mfma_f32_16x16x32_bf16 v[42:45], a[208:211], v[62:65], v[42:45]\n"
"v_mfma_f32_16x16x32_bf16 v[46:49], a[216:219], v[62:65], v[46:49]\n"
"v_mfma_f32_16x16x32_bf16 v[50:53], a[224:227], v[62:65], v[50:53]\n"
"v_mfma_f32_16x16x32_bf16 v[54:57], a[232:235], v[62:65], v[54:57]\n"
"s_barrier\n"
"s_waitcnt lgkmcnt(1)\n"
"v_mfma_f32_16x16x32_bf16 v[10:13], a[196:199], v[66:69], v[10:13]\n"
"ds_read_b128 v[58:61], v5 offset:20480\n"
"v_mfma_f32_16x16x32_bf16 v[14:17], a[204:207], v[66:69], v[14:17]\n"
"ds_read_b128 v[62:65], v5 offset:21504\n"
"v_mfma_f32_16x16x32_bf16 v[18:21], a[212:215], v[66:69], v[18:21]\n"
"v_mfma_f32_16x16x32_bf16 v[22:25], a[220:223], v[66:69], v[22:25]\n"
"v_mfma_f32_16x16x32_bf16 v[26:29], a[228:231], v[66:69], v[26:29]\n"
"v_mfma_f32_16x16x32_bf16 v[30:33], a[236:239], v[66:69], v[30:33]\n"
"s_waitcnt lgkmcnt(2)\n"
"v_mfma_f32_16x16x32_bf16 v[34:37], a[196:199], v[70:73], v[34:37]\n"
"v_mfma_f32_16x16x32_bf16 v[38:41], a[204:207], v[70:73], v[38:41]\n"
"v_mfma_f32_16x16x32_bf16 v[42:45], a[212:215], v[70:73], v[42:45]\n"
"v_mfma_f32_16x16x32_bf16 v[46:49], a[220:223], v[70:73], v[46:49]\n"
"v_mfma_f32_16x16x32_bf16 v[50:53], a[228:231], v[70:73], v[50:53]\n"
"v_mfma_f32_16x16x32_bf16 v[54:57], a[236:239], v[70:73], v[54:57]\n"
"s_waitcnt lgkmcnt(1)\n"
"v_mfma_f32_16x16x32_bf16 v[10:13], a[240:243], v[58:61], v[10:13]\n"
"ds_read_b128 v[66:69], v5 offset:22528\n"
"v_mfma_f32_16x16x32_bf16 v[14:17], a[248:251], v[58:61], v[14:17]\n"
"ds_read_b128 v[70:73], v5 offset:23552\n"
"v_mfma_f32_16x16x32_bf16 v[18:21], v[104:107], v[58:61], v[18:21]\n"
"v_mfma_f32_16x16x32_bf16 v[22:25], v[112:115], v[58:61], v[22:25]\n"
"v_mfma_f32_16x16x32_bf16 v[26:29], v[120:123], v[58:61], v[26:29]\n"
"v_mfma_f32_16x16x32_bf16 v[30:33], v[128:131], v[58:61], v[30:33]\n"
"s_waitcnt lgkmcnt(2)\n"
"v_mfma_f32_16x16x32_bf16 v[34:37], a[240:243], v[62:65], v[34:37]\n"
"v_mfma_f32_16x16x32_bf16 v[38:41], a[248:251], v[62:65], v[38:41]\n"
"v_mfma_f32_16x16x32_bf16 v[42:45], v[104:107], v[62:65], v[42:45]\n"
"v_mfma_f32_16x16x32_bf16 v[46:49], v[112:115], v[62:65], v[46:49]\n"
"v_mfma_f32_16x16x32_bf16 v[50:53], v[120:123], v[62:65], v[50:53]\n"
"v_mfma_f32_16x16x32_bf16 v[54:57], v[128:131], v[62:65], v[54:57]\n"
"s_waitcnt lgkmcnt(1)\n"
"v_mfma_f32_16x16x32_bf16 v[10:13], a[244:247], v[66:69], v[10:13]\n"
"ds_read_b128 v[58:61], v5 offset:24576\n"
"v_mfma_f32_16x16x32_bf16 v[14:17], a[252:255], v[66:69], v[14:17]\n"
"ds_read_b128 v[62:65], v5 offset:25600\n"
"v_mfma_f32_16x16x32_bf16 v[18:21], v[108:111], v[66:69], v[18:21]\n"
"v_mfma_f32_16x16x32_bf16 v[22:25], v[116:119], v[66:69], v[22:25]\n"
"v_mfma_f32_16x16x32_bf16 v[26:29], v[124:127], v[66:69], v[26:29]\n"
"v_mfma_f32_16x16x32_bf16 v[30:33], v[132:135], v[66:69], v[30:33]\n"
"s_waitcnt lgkmcnt(2)\n"
"v_mfma_f32_16x16x32_bf16 v[34:37], a[244:247], v[70:73], v[34:37]\n"
"v_mfma_f32_16x16x32_bf16 v[38:41], a[252:255], v[70:73], v[38:41]\n"
"v_mfma_f32_16x16x32_bf16 v[42:45], v[108:111], v[70:73], v[42:45]\n"
"v_mfma_f32_16x16x32_bf16 v[46:49], v[116:119], v[70:73], v[46:49]\n"
"v_mfma_f32_16x16x32_bf16 v[50:53], v[124:127], v[70:73], v[50:53]\n"
"v_mfma_f32_16x16x32_bf16 v[54:57], v[132:135], v[70:73], v[54:57]\n"
"s_waitcnt lgkmcnt(1)\n"
"v_mfma_f32_16x16x32_bf16 v[10:13], v[136:139], v[58:61], v[10:13]\n"
"ds_read_b128 v[66:69], v5 offset:26624\n"
"v_mfma_f32_16x16x32_bf16 v[14:17], v[144:147], v[58:61], v[14:17]\n"
"ds_read_b128 v[70:73], v5 offset:27648\n"
"v_mfma_f32_16x16x32_bf16 v[18:21], v[152:155], v[58:61], v[18:21]\n"
"v_mfma_f32_16x16x32_bf16 v[22:25], v[160:163], v[58:61], v[22:25]\n"
"v_mfma_f32_16x16x32_bf16 v[26:29], v[168:171], v[58:61], v[26:29]\n"
"v_mfma_f32_16x16x32_bf16 v[30:33], v[176:179], v[58:61], v[30:33]\n"
"s_waitcnt lgkmcnt(2)\n"
"v_mfma_f32_16x16x32_bf16 v[34:37], v[136:139], v[62:65], v[34:37]\n"
"v_mfma_f32_16x16x32_bf16 v[38:41], v[144:147], v[62:65], v[38:41]\n"
"v_mfma_f32_16x16x32_bf16 v[42:45], v[152:155], v[62:65], v[42:45]\n"
"v_mfma_f32_16x16x32_bf16 v[46:49], v[160:163], v[62:65], v[46:49]\n"
"v_mfma_f32_16x16x32_bf16 v[50:53], v[168:171], v[62:65], v[50:53]\n"
"v_mfma_f32_16x16x32_bf16 v[54:57], v[176:179], v[62:65], v[54:57]\n"
"s_waitcnt lgkmcnt(1)\n"
"v_mfma_f32_16x16x32_bf16 v[10:13], v[140:143], v[66:69], v[10:13]\n"
"ds_read_b128 v[58:61], v5 offset:28672\n"
"v_mfma_f32_16x16x32_bf16 v[14:17], v[148:151], v[66:69], v[14:17]\n"
"ds_read_b128 v[62:65], v5 offset:29696\n"
"v_mfma_f32_16x16x32_bf16 v[18:21], v[156:159], v[66:69], v[18:21]\n"
"v_mfma_f32_16x16x32_bf16 v[22:25], v[164:167], v[66:69], v[22:25]\n"
"v_mfma_f32_16x16x32_bf16 v[26:29], v[172:175], v[66:69], v[26:29]\n"
"v_mfma_f32_16x16x32_bf16 v[30:33], v[180:183], v[66:69], v[30:33]\n"
"s_waitcnt lgkmcnt(2)\n"
"v_mfma_f32_16x16x32_bf16 v[34:37], v[140:143], v[70:73], v[34:37]\n"
"v_mfma_f32_16x16x32_bf16 v[38:41], v[148:151], v[70:73], v[38:41]\n"
"v_mfma_f32_16x16x32_bf16 v[42:45], v[156:159], v[70:73], v[42:45]\n"
"v_mfma_f32_16x16x32_bf16 v[46:49], v[164:167], v[70:73], v[46:49]\n"
"v_mfma_f32_16x16x32_bf16 v[50:53], v[172:175], v[70:73], v[50:53]\n"
"v_mfma_f32_16x16x32_bf16 v[54:57], v[180:183], v[70:73], v[54:57]\n"
"s_waitcnt lgkmcnt(1)\n"
"v_mfma_f32_16x16x32_bf16 v[10:13], v[184:187], v[58:61], v[10:13]\n"
"ds_read_b128 v[66:69], v5 offset:30720\n"
"v_mfma_f32_16x16x32_bf16 v[14:17], v[192:195], v[58:61], v[14:17]\n"
"ds_read_b128 v[70:73], v5 offset:31744\n"
"v_mfma_f32_16x16x32_bf16 v[18:21], v[200:203], v[58:61], v[18:21]\n"
"v_mfma_f32_16x16x32_bf16 v[22:25], v[208:211], v[58:61], v[22:25]\n"
"v_mfma_f32_16x16x32_bf16 v[26:29], v[216:219], v[58:61], v[26:29]\n"
"v_mfma_f32_16x16x32_bf16 v[30:33], v[224:227], v[58:61], v[30:33]\n"
"s_waitcnt lgkmcnt(2)\n"
"v_mfma_f32_16x16x32_bf16 v[34:37], v[184:187], v[62:65], v[34:37]\n"
"v_mfma_f32_16x16x32_bf16 v[38:41], v[192:195], v[62:65], v[38:41]\n"
"v_mfma_f32_16x16x32_bf16 v[42:45], v[200:203], v[62:65], v[42:45]\n"
"v_mfma_f32_16x16x32_bf16 v[46:49], v[208:211], v[62:65], v[46:49]\n"
"v_mfma_f32_16x16x32_bf16 v[50:53], v[216:219], v[62:65], v[50:53]\n"
"v_mfma_f32_16x16x32_bf16 v[54:57], v[224:227], v[62:65], v[54:57]\n"
"s_waitcnt lgkmcnt(1)\n"
"v_mfma_f32_16x16x32_bf16 v[10:13], v[188:191], v[66:69], v[10:13]\n"
"v_mfma_f32_16x16x32_bf16 v[14:17], v[196:199], v[66:69], v[14:17]\n"
"v_mfma_f32_16x16x32_bf16 v[18:21], v[204:207], v[66:69], v[18:21]\n"
"v_mfma_f32_16x16x32_bf16 v[22:25], v[212:215], v[66:69], v[22:25]\n"
"v_mfma_f32_16x16x32_bf16 v[26:29], v[220:223], v[66:69], v[26:29]\n"
"v_mfma_f32_16x16x32_bf16 v[30:33], v[228:231], v[66:69], v[30:33]\n"
"s_waitcnt lgkmcnt(0)\n"
"v_mfma_f32_16x16x32_bf16 v[34:37], v[188:191], v[70:73], v[34:37]\n"
"v_mfma_f32_16x16x32_bf16 v[38:41], v[196:199], v[70:73], v[38:41]\n"
"v_mfma_f32_16x16x32_bf16 v[42:45], v[204:207], v[70:73], v[42:45]\n"
"v_mfma_f32_16x16x32_bf16 v[46:49], v[212:215], v[70:73], v[46:49]\n"
"v_mfma_f32_16x16x32_bf16 v[50:53], v[220:223], v[70:73], v[50:53]\n"
"v_mfma_f32_16x16x32_bf16 v[54:57], v[228:231], v[70:73], v[54:57]\n"
"s_lshr_b32 s27, s22, 1\n"
"s_mul_i32 s27, s27, 12288\n"
"s_and_b32 s28, s22, 1\n"
"s_mul_i32 s28, s28, 3072\n"
"s_add_u32 s27, s27, s28\n"
"v_add_u32 v98, s27, v5\n"
"v_cvt_pk_bf16_f32 v58, v10, v11\n"
"v_cvt_pk_bf16_f32 v59, v12, v13\n"
"v_cvt_pk_bf16_f32 v60, v14, v15\n"
"v_cvt_pk_bf16_f32 v61, v16, v17\n"
"ds_write_b128 v98, v[58:61] offset:40960\n"
"v_cvt_pk_bf16_f32 v62, v18, v19\n"
"v_cvt_pk_bf16_f32 v63, v20, v21\n"
"v_cvt_pk_bf16_f32 v64, v22, v23\n"
"v_cvt_pk_bf16_f32 v65, v24, v25\n"
"ds_write_b128 v98, v[62:65] offset:41984\n"
"v_cvt_pk_bf16_f32 v66, v26, v27\n"
"v_cvt_pk_bf16_f32 v67, v28, v29\n"
"v_cvt_pk_bf16_f32 v68, v30, v31\n"
"v_cvt_pk_bf16_f32 v69, v32, v33\n"
"ds_write_b128 v98, v[66:69] offset:43008\n"
"v_cvt_pk_bf16_f32 v70, v34, v35\n"
"v_cvt_pk_bf16_f32 v71, v36, v37\n"
"v_cvt_pk_bf16_f32 v72, v38, v39\n"
"v_cvt_pk_bf16_f32 v73, v40, v41\n"
"ds_write_b128 v98, v[70:73] offset:47104\n"
"v_cvt_pk_bf16_f32 v58, v42, v43\n"
"v_cvt_pk_bf16_f32 v59, v44, v45\n"
"v_cvt_pk_bf16_f32 v60, v46, v47\n"
"v_cvt_pk_bf16_f32 v61, v48, v49\n"
"ds_write_b128 v98, v[58:61] offset:48128\n"
"v_cvt_pk_bf16_f32 v62, v50, v51\n"
"v_cvt_pk_bf16_f32 v63, v52, v53\n"
"v_cvt_pk_bf16_f32 v64, v54, v55\n"
"v_cvt_pk_bf16_f32 v65, v56, v57\n"
"ds_write_b128 v98, v[62:65] offset:49152\n"
"s_waitcnt lgkmcnt(0)\n"
"s_barrier\n"
"s_cmp_ge_u32 s25, 62\n"
"s_cbranch_scc1 .La_nox_6\n"
"s_mul_i32 s27, s22, 10240\n"
"s_add_u32 s27, s27, 73728\n"
"s_mov_b32 m0, s27\n"
"s_add_u32 s28, s96, 0x0\n"
"s_addc_u32 s29, s97, 0\n"
"global_load_lds_dwordx4 v0, s[28:29] offset:0 sc1\n"
"global_load_lds_dwordx4 v0, s[28:29] offset:1024 sc1\n"
"global_load_lds_dwordx4 v0, s[28:29] offset:2048 sc1\n"
"global_load_lds_dwordx4 v0, s[28:29] offset:3072 sc1\n"
"s_mul_i32 s27, s22, 10240\n"
"s_add_u32 s27, s27, 77824\n"
"s_mov_b32 m0, s27\n"
"s_add_u32 s28, s96, 0x1000\n"
"s_addc_u32 s29, s97, 0\n"
"global_load_lds_dwordx4 v0, s[28:29] offset:0 sc1\n"
"global_load_lds_dwordx4 v0, s[28:29] offset:1024 sc1\n"
"global_load_lds_dwordx4 v0, s[28:29] offset:2048 sc1\n"
"global_load_lds_dwordx4 v0, s[28:29] offset:3072 sc1\n"
"s_mul_i32 s27, s22, 10240\n"
"s_add_u32 s27, s27, 81920\n"
"s_mov_b32 m0, s27\n"
"s_add_u32 s28, s96, 0x2000\n"
"s_addc_u32 s29, s97, 0\n"
"global_load_lds_dwordx4 v0, s[28:29] offset:0 sc1\n"
"global_load_lds_dwordx4 v0, s[28:29] offset:1024 sc1\n"
".La_nox_6:\n"
"s_add_u32 s96, s96, 0x14000\n"
"s_addc_u32 s97, s97, 0\n"
"ds_read_b128 v[74:77], v4 offset:12288\n"
"ds_read_b128 v[78:81], v4 offset:13312\n"
"ds_read_b128 v[82:85], v4 offset:14336\n"
"ds_read_b128 v[86:89], v4 offset:15360\n"
"ds_read_b128 v[90:93], v4 offset:16384\n"
"ds_read_b128 v[94:97], v4 offset:17408\n"
"v_mov_b64 v[50:51], 0\n"
"v_mov_b64 v[52:53], 0\n"
"v_mov_b64 v[54:55], 0\n"
"v_mov_b64 v[56:57], 0\n"
"v_mov_b64 v[58:59], 0\n"
"v_mov_b64 v[60:61], 0\n"
"v_mov_b64 v[62:63], 0\n"
"v_mov_b64 v[64:65], 0\n"
"v_mov_b64 v[66:67], 0\n"
"v_mov_b64 v[68:69], 0\n"
"v_mov_b64 v[70:71], 0\n"
"v_mov_b64 v[72:73], 0\n"
"v_mov_b32 v102, 0xff800000\n"
"v_cndmask_b32 v98, v102, 0, s[40:41]\n"
"v_cndmask_b32 v99, v102, 0, s[40:41]\n"
"v_cndmask_b32 v100, v102, 0, s[40:41]\n"
"v_cndmask_b32 v101, v102, 0, s[40:41]\n"
"v_mov_b32 v103, 0xd04e79ff\n"
"s_lshl_b32 s27, s22, 11\n"
"s_min_u32 s27, s27, 5120\n"
"s_add_u32 s28, s27, 0x22000\n"
"v_add_u32 v9, s28, v0\n"
"s_add_u32 s27, s27, 3072\n"
"s_cmp_ge_u32 s27, 6144\n"
"s_cselect_b32 s28, 10240, 0\n"
"s_add_u32 s28, s27, s28\n"
"s_add_u32 s60, s30, s28\n"
"s_addc_u32 s61, s31, 0\n"
"s_add_u32 s27, s27, 1024\n"
"s_cmp_ge_u32 s27, 6144\n"
"s_cselect_b32 s28, 10240, 0\n"
"s_add_u32 s27, s27, s28\n"
"s_add_u32 s28, s30, s27\n"
"s_addc_u32 s29, s31, 0\n"
"s_add_u32 s70, s25, 1\n"
"s_cmp_lg_u32 s22, 0\n"
"s_cbranch_scc1 .La_tkok_8\n"
"v_mov_b32 v25, 0\n"
"global_load_dword v22, v25, s[98:99] sc1\n"
".La_tk_7:\n"
"global_load_dword v23, v25, s[98:99] sc1\n"
"s_waitcnt vmcnt(1)\n"
"v_cmp_gt_u32 vcc, s70, v22\n"
"s_cbranch_vccz .La_tkok_8\n"
"global_load_dword v22, v25, s[98:99] sc1\n"
"s_waitcnt vmcnt(1)\n"
"v_cmp_gt_u32 vcc, s70, v23\n"
"s_cbranch_vccz .La_tkok_8\n"
"s_add_u32 s26, s26, 1\n"
"s_cmp_lt_u32 s26, 0x80000\n"
"s_cbranch_scc1 .La_tk_7\n"
"s_branch .La_abort\n"
".La_tkok_8:\n"
"s_barrier\n"
"global_load_dwordx4 v[10:13], v0, s[30:31] sc1\n"
"s_nop 9\n"
"global_load_dwordx4 v[14:17], v0, s[30:31] offset:1024 sc1\n"
"s_nop 9\n"
"global_load_dwordx4 v[42:45], v0, s[60:61] sc1\n"
"s_nop 9\n"
"global_load_dwordx4 v[46:49], v0, s[28:29] sc1\n"
"s_nop 9\n"
"global_load_dwordx4 v[18:21], v0, s[30:31] offset:2048 sc1\n"
".La_ww_9:\n"
"v_mov_b32 v102, v102\n"
"s_getreg_b32 s27, hwreg(HW_REG_IB_STS, 0, 4)\n"
"s_cmp_gt_u32 s27, 4\n"
"s_cbranch_scc1 .La_ww_9\n"
"s_waitcnt vmcnt(4)\n"
"v_smfmac_f32_16x16x64_bf16 v[50:53], v[10:13], a[0:7], v1\n"
"v_smfmac_f32_16x16x64_bf16 v[54:57], v[10:13], a[8:15], v1\n"
"v_max_u32 v102, v10, v12\n"
"v_smfmac_f32_16x16x64_bf16 v[58:61], v[10:13], a[16:23], v1\n"
"v_smfmac_f32_16x16x64_bf16 v[62:65], v[10:13], a[24:31], v1\n"
"v_smfmac_f32_16x16x64_bf16 v[66:69], v[10:13], a[32:39], v1\n"
"v_smfmac_f32_16x16x64_bf16 v[70:73], v[10:13], a[40:47], v1\n"
"s_waitcnt vmcnt(3)\n"
"v_smfmac_f32_16x16x64_bf16 v[50:53], v[14:17], a[48:55], v1\n"
"v_smfmac_f32_16x16x64_bf16 v[54:57], v[14:17], a[56:63], v1\n"
"s_waitcnt vmcnt(2)\n"
"ds_write_b128 v9, v[42:45]\n"
"v_smfmac_f32_16x16x64_bf16 v[58:61], v[14:17], a[64:71], v1\n"
"v_max3_u32 v102, v102, v14, v16\n"
"v_smfmac_f32_16x16x64_bf16 v[62:65], v[14:17], a[72:79], v1\n"
"v_smfmac_f32_16x16x64_bf16 v[66:69], v[14:17], a[80:87], v1\n"
"v_smfmac_f32_16x16x64_bf16 v[70:73], v[14:17], a[88:95], v1\n"
"s_waitcnt vmcnt(1)\n"
"ds_write_b128 v9, v[46:49] offset:1024\n"
"s_waitcnt vmcnt(0)\n"
"v_smfmac_f32_16x16x64_bf16 v[50:53], v[18:21], a[96:103], v1\n"
"v_smfmac_f32_16x16x64_bf16 v[54:57], v[18:21], a[104:111], v1\n"
"v_max3_u32 v102, v102, v18, v20\n"
"v_smfmac_f32_16x16x64_bf16 v[58:61], v[18:21], a[112:119], v1\n"
"s_waitcnt lgkmcnt(0)\n"
"s_barrier\n"
"ds_read_b128 v[22:25], v6\n"
"v_smfmac_f32_16x16x64_bf16 v[62:65], v[18:21], a[120:127], v1\n"
"ds_read_b128 v[26:29], v6 offset:1024\n"
"v_smfmac_f32_16x16x64_bf16 v[66:69], v[18:21], a[128:135], v1\n"
"ds_read_b128 v[30:33], v6 offset:2048\n"
"v_smfmac_f32_16x16x64_bf16 v[70:73], v[18:21], a[136:143], v1\n"
"ds_read_b128 v[34:37], v6 offset:3072\n"
"s_waitcnt lgkmcnt(3)\n"
"v_smfmac_f32_16x16x64_bf16 v[50:53], v[22:25], a[144:151], v1\n"
"ds_read_b128 v[38:41], v6 offset:4096\n"
"v_smfmac_f32_16x16x64_bf16 v[54:57], v[22:25], a[152:159], v1\n"
"ds_read_b128 v[42:45], v6 offset:5120\n"
"v_smfmac_f32_16x16x64_bf16 v[58:61], v[22:25], a[160:167], v1\n"
"ds_read_b128 v[46:49], v6 offset:6144\n"
"v_smfmac_f32_16x16x64_bf16 v[62:65], v[22:25], a[168:175], v1\n"
"v_max3_u32 v102, v102, v22, v24\n"
"v_smfmac_f32_16x16x64_bf16 v[66:69], v[22:25], a[176:183], v1\n"
"s_waitcnt lgkmcnt(5)\n"
"v_max3_u32 v102, v102, v26, v28\n"
"v_smfmac_f32_16x16x64_bf16 v[70:73], v[22:25], a[184:191], v1\n"
"s_waitcnt lgkmcnt(4)\n"
"v_max3_u32 v102, v102, v30, v32\n"
"v_smfmac_f32_16x16x64_bf16 v[50:53], v[26:29], a[192:199], v1\n"
"s_waitcnt lgkmcnt(3)\n"
"v_max3_u32 v102, v102, v34, v36\n"
"v_smfmac_f32_16x16x64_bf16 v[54:57], v[26:29], a[200:207], v1\n"
"s_waitcnt lgkmcnt(2)\n"
"v_max3_u32 v102, v102, v38, v40\n"
"v_smfmac_f32_16x16x64_bf16 v[58:61], v[26:29], a[208:215], v1\n"
"s_waitcnt lgkmcnt(1)\n"
"v_max3_u32 v102, v102, v42, v44\n"
"v_smfmac_f32_16x16x64_bf16 v[62:65], v[26:29], a[216:223], v1\n"
"s_waitcnt lgkmcnt(0)\n"
"v_max3_u32 v102, v102, v46, v48\n"
"v_smfmac_f32_16x16x64_bf16 v[66:69], v[26:29], a[224:231], v1\n"
"v_smfmac_f32_16x16x64_bf16 v[70:73], v[26:29], a[232:239], v1\n"
"ds_read_b128 v[10:13], v4 offset:0\n"
"v_smfmac_f32_16x16x64_bf16 v[50:53], v[30:33], a[240:247], v1\n"
"ds_read_b128 v[14:17], v4 offset:1024\n"
"v_smfmac_f32_16x16x64_bf16 v[54:57], v[30:33], a[248:255], v1\n"
"ds_read_b128 v[18:21], v4 offset:6144\n"
"v_smfmac_f32_16x16x64_bf16 v[58:61], v[30:33], v[104:111], v1\n"
"ds_read_b128 v[22:25], v4 offset:7168\n"
"v_smfmac_f32_16x16x64_bf16 v[62:65], v[30:33], v[112:119], v1\n"
"v_smfmac_f32_16x16x64_bf16 v[66:69], v[30:33], v[120:127], v1\n"
"v_smfmac_f32_16x16x64_bf16 v[70:73], v[30:33], v[128:135], v1\n"
"v_cmp_eq_u32 vcc, -1, v102\n"
"s_cbranch_vccnz .La_mfail_10\n"
".La_mtail_11:\n"
"v_smfmac_f32_16x16x64_bf16 v[62:65], v[34:37], v[160:167], v1\n"
"ds_read_b128 v[26:29], v4 offset:2048\n"
"v_smfmac_f32_16x16x64_bf16 v[62:65], v[38:41], v[208:215], v1\n"
"ds_read_b128 v[30:33], v4 offset:3072\n"
"v_smfmac_f32_16x16x64_bf16 v[62:65], v[42:45], v[232:239], v1\n"
"v_smfmac_f32_16x16x64_bf16 v[62:65], v[46:49], v[74:81], v1\n"
"v_smfmac_f32_16x16x64_bf16 v[66:69], v[34:37], v[168:175], v1\n"
"ds_read_b128 v[74:77], v4 offset:8192\n"
"v_smfmac_f32_16x16x64_bf16 v[66:69], v[38:41], v[216:223], v1\n"
"ds_read_b128 v[78:81], v4 offset:9216\n"
"v_smfmac_f32_16x16x64_bf16 v[66:69], v[42:45], v[240:247], v1\n"
"v_smfmac_f32_16x16x64_bf16 v[66:69], v[46:49], v[82:89], v1\n"
"s_nop 1\n"
"v_add_f32 v62, v62, v63\n"
"v_add_f32 v64, v64, v65\n"
"v_smfmac_f32_16x16x64_bf16 v[70:73], v[34:37], v[176:183], v1\n"
"ds_read_b128 v[82:85], v4 offset:4096\n"
"v_cvt_pk_bf16_f32 v62, v62, v64\n"
"v_smfmac_f32_16x16x64_bf16 v[70:73], v[38:41], v[224:231], v1\n"
"ds_read_b128 v[86:89], v4 offset:5120\n"
"ds_write_b32 v2, v62 offset:768\n"
"v_smfmac_f32_16x16x64_bf16 v[70:73], v[42:45], v[248:255], v1\n"
"v_smfmac_f32_16x16x64_bf16 v[70:73], v[46:49], v[90:97], v1\n"
"v_add_f32 v66, v66, v67\n"
"v_add_f32 v68, v68, v69\n"
"v_smfmac_f32_16x16x64_bf16 v[50:53], v[34:37], v[136:143], v1\n"
"ds_read_b128 v[90:93], v4 offset:10240\n"
"v_cvt_pk_bf16_f32 v66, v66, v68\n"
"v_smfmac_f32_16x16x64_bf16 v[50:53], v[38:41], v[184:191], v1\n"
"ds_read_b128 v[94:97], v4 offset:11264\n"
"ds_write_b32 v2, v66 offset:1024\n"
"s_waitcnt lgkmcnt(12)\n"
"v_smfmac_f32_16x16x64_bf16 v[50:53], v[42:45], v[10:17], v1\n"
"s_waitcnt lgkmcnt(10)\n"
"v_smfmac_f32_16x16x64_bf16 v[50:53], v[46:49], v[18:25], v1\n"
"v_add_f32 v70, v70, v71\n"
"v_add_f32 v72, v72, v73\n"
"v_smfmac_f32_16x16x64_bf16 v[54:57], v[34:37], v[144:151], v1\n"
"v_cvt_pk_bf16_f32 v70, v70, v72\n"
"ds_write_b32 v2, v70 offset:1280\n"
"ds_read_b128 v[62:65], v5 offset:53248\n"
"v_smfmac_f32_16x16x64_bf16 v[54:57], v[38:41], v[192:199], v1\n"
"ds_read_b128 v[66:69], v5 offset:59392\n"
"ds_read_b128 v[10:13], v5 offset:54272\n"
"ds_read_b128 v[14:17], v5 offset:60416\n"
"s_waitcnt lgkmcnt(13)\n"
"v_smfmac_f32_16x16x64_bf16 v[54:57], v[42:45], v[26:33], v1\n"
"ds_read_b128 v[18:21], v5 offset:55296\n"
"ds_read_b128 v[22:25], v5 offset:61440\n"
"s_waitcnt lgkmcnt(13)\n"
"v_smfmac_f32_16x16x64_bf16 v[54:57], v[46:49], v[74:81], v1\n"
"v_add_f32 v50, v50, v51\n"
"v_add_f32 v52, v52, v53\n"
"v_smfmac_f32_16x16x64_bf16 v[58:61], v[34:37], v[152:159], v1\n"
"v_cvt_pk_bf16_f32 v50, v50, v52\n"
"ds_write_b32 v2, v50 offset:0\n"
"v_smfmac_f32_16x16x64_bf16 v[58:61], v[38:41], v[200:207], v1\n"
"s_waitcnt lgkmcnt(12)\n"
"v_smfmac_f32_16x16x64_bf16 v[58:61], v[42:45], v[82:89], v1\n"
"s_waitcnt lgkmcnt(9)\n"
"v_smfmac_f32_16x16x64_bf16 v[58:61], v[46:49], v[90:97], v1\n"
"v_add_f32 v54, v54, v55\n"
"v_add_f32 v56, v56, v57\n"
"v_cvt_pk_bf16_f32 v54, v54, v56\n"
"ds_write_b32 v2, v54 offset:256\n"
"s_nop 3\n"
"v_add_f32 v58, v58, v59\n"
"v_add_f32 v60, v60, v61\n"
"v_cvt_pk_bf16_f32 v58, v58, v60\n"
"ds_write_b32 v2, v58 offset:512\n"
"s_waitcnt lgkmcnt(0)\n"
"s_barrier\n"
"s_cmp_lg_u32 s22, 0\n"
"s_cbranch_scc1 .La_wx_16\n"
"ds_read_b64_tr_b16 v[42:43], v3 offset:0\n"
"ds_read_b64_tr_b16 v[44:45], v3 offset:256\n"
"ds_read_b64_tr_b16 v[74:75], v3 offset:3072\n"
"ds_read_b64_tr_b16 v[76:77], v3 offset:3328\n"
"ds_read_b64_tr_b16 v[46:47], v3 offset:512\n"
"ds_read_b64_tr_b16 v[48:49], v3 offset:768\n"
"v_mov_b32 v103, s70\n"
"v_lshlrev_b32 v102, 4, v0\n"
"s_waitcnt lgkmcnt(4)\n"
"v_mfma_f32_16x16x32_bf16 v[90:93], v[62:65], v[42:45], 0\n"
"ds_read_b64_tr_b16 v[78:79], v3 offset:3584\n"
"ds_read_b64_tr_b16 v[80:81], v3 offset:3840\n"
"ds_read_b64_tr_b16 v[50:51], v3 offset:1024\n"
"v_mfma_f32_16x16x32_bf16 v[94:97], v[66:69], v[42:45], 0\n"
"ds_read_b64_tr_b16 v[52:53], v3 offset:1280\n"
"ds_read_b64_tr_b16 v[82:83], v3 offset:4096\n"
"ds_read_b64_tr_b16 v[84:85], v3 offset:4352\n"
"s_waitcnt lgkmcnt(8)\n"
"v_mfma_f32_16x16x32_bf16 v[98:101], v[74:77], v[42:45], v[98:101]\n"
"ds_read_b64_tr_b16 v[54:55], v3 offset:1536\n"
"ds_read_b64_tr_b16 v[56:57], v3 offset:1792\n"
"ds_read_b128 v[26:29], v5 offset:56320\n"
"s_waitcnt lgkmcnt(9)\n"
"v_mfma_f32_16x16x32_bf16 v[90:93], v[10:13], v[46:49], v[90:93]\n"
"ds_read_b128 v[30:33], v5 offset:62464\n"
"ds_read_b64_tr_b16 v[86:87], v3 offset:4608\n"
"ds_read_b64_tr_b16 v[88:89], v3 offset:4864\n"
"v_mfma_f32_16x16x32_bf16 v[94:97], v[14:17], v[46:49], v[94:97]\n"
"ds_read_b64_tr_b16 v[58:59], v3 offset:2048\n"
"ds_read_b64_tr_b16 v[60:61], v3 offset:2304\n"
"ds_read_b128 v[34:37], v5 offset:57344\n"
"s_waitcnt lgkmcnt(13)\n"
"v_mfma_f32_16x16x32_bf16 v[98:101], v[78:81], v[46:49], v[98:101]\n"
"ds_read_b128 v[38:41], v5 offset:63488\n"
"s_waitcnt lgkmcnt(12)\n"
"v_mfma_f32_16x16x32_bf16 v[90:93], v[18:21], v[50:53], v[90:93]\n"
"ds_read_b64_tr_b16 v[10:11], v3 offset:5120\n"
"ds_read_b64_tr_b16 v[12:13], v3 offset:5376\n"
"ds_read_b64_tr_b16 v[70:71], v3 offset:2560\n"
"v_mfma_f32_16x16x32_bf16 v[94:97], v[22:25], v[50:53], v[94:97]\n"
"ds_read_b64_tr_b16 v[72:73], v3 offset:2816\n"
"ds_read_b128 v[62:65], v5 offset:58368\n"
"ds_read_b128 v[66:69], v5 offset:64512\n"
"s_waitcnt lgkmcnt(15)\n"
"v_mfma_f32_16x16x32_bf16 v[98:101], v[82:85], v[50:53], v[98:101]\n"
"ds_read_b64_tr_b16 v[14:15], v3 offset:5632\n"
"ds_read_b64_tr_b16 v[16:17], v3 offset:5888\n"
"s_waitcnt lgkmcnt(15)\n"
"v_mfma_f32_16x16x32_bf16 v[90:93], v[26:29], v[54:57], v[90:93]\n"
"s_waitcnt lgkmcnt(14)\n"
"v_mfma_f32_16x16x32_bf16 v[94:97], v[30:33], v[54:57], v[94:97]\n"
"s_waitcnt lgkmcnt(12)\n"
"v_mfma_f32_16x16x32_bf16 v[98:101], v[86:89], v[54:57], v[98:101]\n"
"s_waitcnt lgkmcnt(9)\n"
"v_mfma_f32_16x16x32_bf16 v[90:93], v[34:37], v[58:61], v[90:93]\n"
"s_waitcnt lgkmcnt(8)\n"
"v_mfma_f32_16x16x32_bf16 v[94:97], v[38:41], v[58:61], v[94:97]\n"
"s_waitcnt lgkmcnt(6)\n"
"v_mfma_f32_16x16x32_bf16 v[98:101], v[10:13], v[58:61], v[98:101]\n"
"s_waitcnt lgkmcnt(3)\n"
"v_mfma_f32_16x16x32_bf16 v[90:93], v[62:65], v[70:73], v[90:93]\n"
"s_waitcnt lgkmcnt(2)\n"
"v_mfma_f32_16x16x32_bf16 v[94:97], v[66:69], v[70:73], v[94:97]\n"
"s_waitcnt lgkmcnt(0)\n"
"v_mfma_f32_16x16x32_bf16 v[98:101], v[14:17], v[70:73], v[98:101]\n"
"s_nop 3\n"
"v_cndmask_b32 v10, v90, v92, s[42:43]\n"
"v_cndmask_b32 v11, v91, v93, s[42:43]\n"
"v_cndmask_b32 v12, v94, v96, s[42:43]\n"
"v_cndmask_b32 v13, v95, v97, s[42:43]\n"
"v_cndmask_b32 v14, v98, v100, s[42:43]\n"
"s_mov_b64 exec, s[46:47]\n"
"s_cbranch_execz .La_nt_17\n"
"global_store_dword v102, v103, s[58:59] sc1\n"
".La_nt_17:\n"
"s_mov_b64 exec, -1\n"
"v_cndmask_b32 v15, v99, v101, s[42:43]\n"
"v_max3_f32 v16, v10, v11, v12\n"
"v_max3_f32 v16, v16, v13, v14\n"
"v_max_f32 v16, v16, v15\n"
"v_mul_f32 v18, s71, v16\n"
"v_mov_b32 v24, s69\n"
"v_max_f32_dpp v20, v16, v16 row_ror:8 row_mask:0xf bank_mask:0xf\n"
"v_pk_fma_f32 v[10:11], v[10:11], v[24:25], v[18:19] op_sel_hi:[1,0,0]\n"
"v_pk_fma_f32 v[12:13], v[12:13], v[24:25], v[18:19] op_sel_hi:[1,0,0]\n"
"v_pk_fma_f32 v[14:15], v[14:15], v[24:25], v[18:19] op_sel_hi:[1,0,0]\n"
"v_mov_b32 v21, v20\n"
"v_exp_f32 v10, v10\n"
"v_exp_f32 v11, v11\n"
"v_permlane16_swap_b32 v20, v21\n"
"v_exp_f32 v12, v12\n"
"v_max_f32 v20, v20, v21\n"
"v_mov_b32 v21, v20\n"
"v_exp_f32 v13, v13\n"
"v_exp_f32 v14, v14\n"
"v_permlane32_swap_b32 v20, v21\n"
"v_exp_f32 v15, v15\n"
"v_max_f32 v20, v20, v21\n"
"v_fma_f32 v20, v20, s71, -v18\n"
"v_pk_add_f32 v[22:23], v[10:11], v[12:13]\n"
"v_exp_f32 v20, v20\n"
"v_pk_add_f32 v[22:23], v[22:23], v[14:15]\n"
"v_add_f32 v22, v22, v23\n"
"v_mul_f32 v22, v22, v20\n"
"v_pk_mul_f32 v[10:11], v[10:11], v[20:21] op_sel_hi:[1,0]\n"
"v_pk_mul_f32 v[12:13], v[12:13], v[20:21] op_sel_hi:[1,0]\n"
"v_add_f32_dpp v22, v22, v22 row_ror:8 row_mask:0xf bank_mask:0xf\n"
"v_mov_b32 v23, v22\n"
"v_pk_mul_f32 v[14:15], v[14:15], v[20:21] op_sel_hi:[1,0]\n"
"s_nop 0\n"
"v_permlane16_swap_b32 v22, v23\n"
"v_add_f32 v22, v22, v23\n"
"v_mov_b32 v23, v22\n"
"s_nop 1\n"
"v_permlane32_swap_b32 v22, v23\n"
"v_add_f32 v22, v22, v23\n"
"v_rcp_f32 v22, v22\n"
"s_nop 0\n"
"v_pk_mul_f32 v[10:11], v[10:11], v[22:23] op_sel_hi:[1,0]\n"
"v_pk_mul_f32 v[12:13], v[12:13], v[22:23] op_sel_hi:[1,0]\n"
"v_pk_mul_f32 v[14:15], v[14:15], v[22:23] op_sel_hi:[1,0]\n"
"v_cvt_pk_bf16_f32 v18, v10, v11\n"
"v_cvt_pk_bf16_f32 v19, v12, v13\n"
"global_store_dwordx2 v7, v[18:19], s[34:35] sc1\n"
"v_cvt_pk_bf16_f32 v20, v14, v15\n"
"s_mov_b64 exec, s[40:41]\n"
"global_store_dword v8, v20, s[34:35] offset:-1024 sc1\n"
"s_mov_b64 exec, -1\n"
"s_branch .La_nxt_14\n"
".La_wx_16:\n"
"s_cmp_eq_u32 s22, 2\n"
"s_cbranch_scc1 .La_w2_13\n"
"s_cmp_eq_u32 s22, 3\n"
"s_cbranch_scc1 .La_nxt_14\n"
".La_w1_12:\n"
"s_nop 7\n"
"ds_read_b128 v[42:45], v5 offset:40960\n"
"ds_read_b64_tr_b16 v[74:75], v3 offset:3072\n"
"ds_read_b64_tr_b16 v[76:77], v3 offset:3328\n"
"ds_read_b128 v[46:49], v5 offset:41984\n"
"s_waitcnt lgkmcnt(3)\n"
"v_mfma_f32_16x16x32_bf16 v[90:93], v[62:65], v[42:45], 0\n"
"ds_read_b64_tr_b16 v[78:79], v3 offset:3584\n"
"ds_read_b64_tr_b16 v[80:81], v3 offset:3840\n"
"ds_read_b128 v[50:53], v5 offset:43008\n"
"s_waitcnt lgkmcnt(4)\n"
"v_mfma_f32_16x16x32_bf16 v[98:101], v[74:77], v[42:45], v[98:101]\n"
"ds_read_b64_tr_b16 v[82:83], v3 offset:4096\n"
"ds_read_b64_tr_b16 v[84:85], v3 offset:4352\n"
"ds_read_b128 v[54:57], v5 offset:44032\n"
"s_waitcnt lgkmcnt(6)\n"
"v_mfma_f32_16x16x32_bf16 v[90:93], v[10:13], v[46:49], v[90:93]\n"
"ds_read_b128 v[26:29], v5 offset:56320\n"
"ds_read_b64_tr_b16 v[86:87], v3 offset:4608\n"
"ds_read_b64_tr_b16 v[88:89], v3 offset:4864\n"
"s_waitcnt lgkmcnt(7)\n"
"v_mfma_f32_16x16x32_bf16 v[98:101], v[78:81], v[46:49], v[98:101]\n"
"ds_read_b128 v[58:61], v5 offset:45056\n"
"ds_read_b128 v[34:37], v5 offset:57344\n"
"s_waitcnt lgkmcnt(8)\n"
"v_mfma_f32_16x16x32_bf16 v[90:93], v[18:21], v[50:53], v[90:93]\n"
"ds_read_b64_tr_b16 v[10:11], v3 offset:5120\n"
"ds_read_b64_tr_b16 v[12:13], v3 offset:5376\n"
"ds_read_b128 v[70:73], v5 offset:46080\n"
"s_waitcnt lgkmcnt(9)\n"
"v_mfma_f32_16x16x32_bf16 v[98:101], v[82:85], v[50:53], v[98:101]\n"
"ds_read_b128 v[62:65], v5 offset:58368\n"
"ds_read_b64_tr_b16 v[14:15], v3 offset:5632\n"
"ds_read_b64_tr_b16 v[16:17], v3 offset:5888\n"
"s_waitcnt lgkmcnt(10)\n"
"v_mfma_f32_16x16x32_bf16 v[90:93], v[26:29], v[54:57], v[90:93]\n"
"s_waitcnt lgkmcnt(8)\n"
"v_mfma_f32_16x16x32_bf16 v[98:101], v[86:89], v[54:57], v[98:101]\n"
"s_waitcnt lgkmcnt(6)\n"
"v_mfma_f32_16x16x32_bf16 v[90:93], v[34:37], v[58:61], v[90:93]\n"
"s_waitcnt lgkmcnt(4)\n"
"v_mfma_f32_16x16x32_bf16 v[98:101], v[10:13], v[58:61], v[98:101]\n"
"s_waitcnt lgkmcnt(2)\n"
"v_mfma_f32_16x16x32_bf16 v[90:93], v[62:65], v[70:73], v[90:93]\n"
"s_waitcnt lgkmcnt(0)\n"
"v_mfma_f32_16x16x32_bf16 v[98:101], v[14:17], v[70:73], v[98:101]\n"
"s_nop 5\n"
"v_cndmask_b32 v90, v103, v90, s[48:49]\n"
"v_cndmask_b32 v91, v103, v91, s[50:51]\n"
"v_cndmask_b32 v92, v103, v92, s[52:53]\n"
"v_cndmask_b32 v93, v103, v93, s[54:55]\n"
"v_mov_b32 v94, v103\n"
"v_mov_b32 v95, v103\n"
"v_mov_b32 v96, v103\n"
"v_mov_b32 v97, v103\n"
"v_max3_f32 v16, v90, v91, v92\n"
"v_max3_f32 v16, v16, v93, v94\n"
"v_max3_f32 v16, v16, v95, v96\n"
"v_max3_f32 v16, v16, v97, v98\n"
"v_max3_f32 v16, v16, v99, v100\n"
"v_max_f32 v16, v16, v101\n"
"v_mov_b32 v17, v16\n"
"s_nop 1\n"
"v_permlane16_swap_b32 v16, v17\n"
"v_max_f32 v16, v16, v17\n"
"v_mov_b32 v17, v16\n"
"s_nop 1\n"
"v_permlane32_swap_b32 v16, v17\n"
"v_max_f32 v16, v16, v17\n"
"v_mul_f32 v16, s69, v16\n"
"v_fma_f32 v90, v90, s69, -v16\n"
"v_fma_f32 v91, v91, s69, -v16\n"
"v_fma_f32 v92, v92, s69, -v16\n"
"v_fma_f32 v93, v93, s69, -v16\n"
"v_fma_f32 v94, v94, s69, -v16\n"
"v_fma_f32 v95, v95, s69, -v16\n"
"v_fma_f32 v96, v96, s69, -v16\n"
"v_fma_f32 v97, v97, s69, -v16\n"
"v_fma_f32 v98, v98, s69, -v16\n"
"v_fma_f32 v99, v99, s69, -v16\n"
"v_fma_f32 v100, v100, s69, -v16\n"
"v_fma_f32 v101, v101, s69, -v16\n"
"v_exp_f32 v90, v90\n"
"v_exp_f32 v91, v91\n"
"v_exp_f32 v92, v92\n"
"v_exp_f32 v93, v93\n"
"v_exp_f32 v94, v94\n"
"v_exp_f32 v95, v95\n"
"v_exp_f32 v96, v96\n"
"v_exp_f32 v97, v97\n"
"v_exp_f32 v98, v98\n"
"v_exp_f32 v99, v99\n"
"v_exp_f32 v100, v100\n"
"v_exp_f32 v101, v101\n"
"v_add_f32 v16, v90, v91\n"
"v_add_f32 v17, v92, v93\n"
"v_add_f32 v16, v16, v94\n"
"v_add_f32 v17, v17, v95\n"
"v_add_f32 v16, v16, v96\n"
"v_add_f32 v17, v17, v97\n"
"v_add_f32 v16, v16, v98\n"
"v_add_f32 v17, v17, v99\n"
"v_add_f32 v16, v16, v100\n"
"v_add_f32 v17, v17, v101\n"
"v_add_f32 v16, v16, v17\n"
"v_mov_b32 v17, v16\n"
"s_nop 1\n"
"v_permlane16_swap_b32 v16, v17\n"
"v_add_f32 v16, v16, v17\n"
"v_mov_b32 v17, v16\n"
"s_nop 1\n"
"v_permlane32_swap_b32 v16, v17\n"
"v_add_f32 v16, v16, v17\n"
"v_rcp_f32 v16, v16\n"
"s_nop 0\n"
"v_mul_f32 v90, v90, v16\n"
"v_mul_f32 v91, v91, v16\n"
"v_mul_f32 v92, v92, v16\n"
"v_mul_f32 v93, v93, v16\n"
"v_mul_f32 v94, v94, v16\n"
"v_mul_f32 v95, v95, v16\n"
"v_mul_f32 v96, v96, v16\n"
"v_mul_f32 v97, v97, v16\n"
"v_mul_f32 v98, v98, v16\n"
"v_mul_f32 v99, v99, v16\n"
"v_mul_f32 v100, v100, v16\n"
"v_mul_f32 v101, v101, v16\n"
"v_cvt_pk_bf16_f32 v18, v90, v91\n"
"v_cvt_pk_bf16_f32 v19, v92, v93\n"
"v_cvt_pk_bf16_f32 v20, v94, v95\n"
"v_cvt_pk_bf16_f32 v21, v96, v97\n"
"v_cvt_pk_bf16_f32 v22, v98, v99\n"
"v_cvt_pk_bf16_f32 v23, v100, v101\n"
"global_store_dwordx4 v0, v[18:21], s[36:37] sc1\n"
"s_mov_b64 exec, s[40:41]\n"
"global_store_dwordx2 v8, v[22:23], s[36:37] sc1\n"
"s_mov_b64 exec, -1\n"
"s_branch .La_nxt_14\n"
".La_w2_13:\n"
"s_nop 7\n"
"ds_read_b128 v[42:45], v5 offset:47104\n"
"ds_read_b64_tr_b16 v[74:75], v3 offset:3072\n"
"ds_read_b64_tr_b16 v[76:77], v3 offset:3328\n"
"ds_read_b128 v[46:49], v5 offset:48128\n"
"s_waitcnt lgkmcnt(3)\n"
"v_mfma_f32_16x16x32_bf16 v[90:93], v[62:65], v[42:45], 0\n"
"ds_read_b64_tr_b16 v[78:79], v3 offset:3584\n"
"ds_read_b64_tr_b16 v[80:81], v3 offset:3840\n"
"ds_read_b128 v[50:53], v5 offset:49152\n"
"v_mfma_f32_16x16x32_bf16 v[94:97], v[66:69], v[42:45], 0\n"
"ds_read_b64_tr_b16 v[82:83], v3 offset:4096\n"
"ds_read_b64_tr_b16 v[84:85], v3 offset:4352\n"
"ds_read_b128 v[54:57], v5 offset:50176\n"
"s_waitcnt lgkmcnt(7)\n"
"v_mfma_f32_16x16x32_bf16 v[98:101], v[74:77], v[42:45], v[98:101]\n"
"ds_read_b128 v[26:29], v5 offset:56320\n"
"ds_read_b128 v[30:33], v5 offset:62464\n"
"ds_read_b64_tr_b16 v[86:87], v3 offset:4608\n"
"s_waitcnt lgkmcnt(9)\n"
"v_mfma_f32_16x16x32_bf16 v[90:93], v[10:13], v[46:49], v[90:93]\n"
"ds_read_b64_tr_b16 v[88:89], v3 offset:4864\n"
"ds_read_b128 v[58:61], v5 offset:51200\n"
"ds_read_b128 v[34:37], v5 offset:57344\n"
"v_mfma_f32_16x16x32_bf16 v[94:97], v[14:17], v[46:49], v[94:97]\n"
"ds_read_b128 v[38:41], v5 offset:63488\n"
"s_waitcnt lgkmcnt(11)\n"
"v_mfma_f32_16x16x32_bf16 v[98:101], v[78:81], v[46:49], v[98:101]\n"
"s_waitcnt lgkmcnt(10)\n"
"v_mfma_f32_16x16x32_bf16 v[90:93], v[18:21], v[50:53], v[90:93]\n"
"ds_read_b64_tr_b16 v[10:11], v3 offset:5120\n"
"ds_read_b64_tr_b16 v[12:13], v3 offset:5376\n"
"ds_read_b128 v[70:73], v5 offset:52224\n"
"v_mfma_f32_16x16x32_bf16 v[94:97], v[22:25], v[50:53], v[94:97]\n"
"ds_read_b128 v[62:65], v5 offset:58368\n"
"ds_read_b128 v[66:69], v5 offset:64512\n"
"ds_read_b64_tr_b16 v[14:15], v3 offset:5632\n"
"s_waitcnt lgkmcnt(14)\n"
"v_mfma_f32_16x16x32_bf16 v[98:101], v[82:85], v[50:53], v[98:101]\n"
"ds_read_b64_tr_b16 v[16:17], v3 offset:5888\n"
"s_waitcnt lgkmcnt(13)\n"
"v_mfma_f32_16x16x32_bf16 v[90:93], v[26:29], v[54:57], v[90:93]\n"
"s_waitcnt lgkmcnt(12)\n"
"v_mfma_f32_16x16x32_bf16 v[94:97], v[30:33], v[54:57], v[94:97]\n"
"s_waitcnt lgkmcnt(10)\n"
"v_mfma_f32_16x16x32_bf16 v[98:101], v[86:89], v[54:57], v[98:101]\n"
"s_waitcnt lgkmcnt(8)\n"
"v_mfma_f32_16x16x32_bf16 v[90:93], v[34:37], v[58:61], v[90:93]\n"
"s_waitcnt lgkmcnt(7)\n"
"v_mfma_f32_16x16x32_bf16 v[94:97], v[38:41], v[58:61], v[94:97]\n"
"s_waitcnt lgkmcnt(5)\n"
"v_mfma_f32_16x16x32_bf16 v[98:101], v[10:13], v[58:61], v[98:101]\n"
"s_waitcnt lgkmcnt(3)\n"
"v_mfma_f32_16x16x32_bf16 v[90:93], v[62:65], v[70:73], v[90:93]\n"
"s_waitcnt lgkmcnt(2)\n"
"v_mfma_f32_16x16x32_bf16 v[94:97], v[66:69], v[70:73], v[94:97]\n"
"s_waitcnt lgkmcnt(0)\n"
"v_mfma_f32_16x16x32_bf16 v[98:101], v[14:17], v[70:73], v[98:101]\n"
"s_nop 5\n"
"v_cndmask_b32 v94, v103, v94, s[48:49]\n"
"v_cndmask_b32 v95, v103, v95, s[50:51]\n"
"v_cndmask_b32 v96, v103, v96, s[52:53]\n"
"v_cndmask_b32 v97, v103, v97, s[54:55]\n"
"v_max3_f32 v16, v90, v91, v92\n"
"v_max3_f32 v16, v16, v93, v94\n"
"v_max3_f32 v16, v16, v95, v96\n"
"v_max3_f32 v16, v16, v97, v98\n"
"v_max3_f32 v16, v16, v99, v100\n"
"v_max_f32 v16, v16, v101\n"
"v_mov_b32 v17, v16\n"
"s_nop 1\n"
"v_permlane16_swap_b32 v16, v17\n"
"v_max_f32 v16, v16, v17\n"
"v_mov_b32 v17, v16\n"
"s_nop 1\n"
"v_permlane32_swap_b32 v16, v17\n"
"v_max_f32 v16, v16, v17\n"
"v_mul_f32 v16, s69, v16\n"
"v_fma_f32 v90, v90, s69, -v16\n"
"v_fma_f32 v91, v91, s69, -v16\n"
"v_fma_f32 v92, v92, s69, -v16\n"
"v_fma_f32 v93, v93, s69, -v16\n"
"v_fma_f32 v94, v94, s69, -v16\n"
"v_fma_f32 v95, v95, s69, -v16\n"
"v_fma_f32 v96, v96, s69, -v16\n"
"v_fma_f32 v97, v97, s69, -v16\n"
"v_fma_f32 v98, v98, s69, -v16\n"
"v_fma_f32 v99, v99, s69, -v16\n"
"v_fma_f32 v100, v100, s69, -v16\n"
"v_fma_f32 v101, v101, s69, -v16\n"
"v_exp_f32 v90, v90\n"
"v_exp_f32 v91, v91\n"
"v_exp_f32 v92, v92\n"
"v_exp_f32 v93, v93\n"
"v_exp_f32 v94, v94\n"
"v_exp_f32 v95, v95\n"
"v_exp_f32 v96, v96\n"
"v_exp_f32 v97, v97\n"
"v_exp_f32 v98, v98\n"
"v_exp_f32 v99, v99\n"
"v_exp_f32 v100, v100\n"
"v_exp_f32 v101, v101\n"
"v_add_f32 v16, v90, v91\n"
"v_add_f32 v17, v92, v93\n"
"v_add_f32 v16, v16, v94\n"
"v_add_f32 v17, v17, v95\n"
"v_add_f32 v16, v16, v96\n"
"v_add_f32 v17, v17, v97\n"
"v_add_f32 v16, v16, v98\n"
"v_add_f32 v17, v17, v99\n"
"v_add_f32 v16, v16, v100\n"
"v_add_f32 v17, v17, v101\n"
"v_add_f32 v16, v16, v17\n"
"v_mov_b32 v17, v16\n"
"s_nop 1\n"
"v_permlane16_swap_b32 v16, v17\n"
"v_add_f32 v16, v16, v17\n"
"v_mov_b32 v17, v16\n"
"s_nop 1\n"
"v_permlane32_swap_b32 v16, v17\n"
"v_add_f32 v16, v16, v17\n"
"v_rcp_f32 v16, v16\n"
"s_nop 0\n"
"v_mul_f32 v90, v90, v16\n"
"v_mul_f32 v91, v91, v16\n"
"v_mul_f32 v92, v92, v16\n"
"v_mul_f32 v93, v93, v16\n"
"v_mul_f32 v94, v94, v16\n"
"v_mul_f32 v95, v95, v16\n"
"v_mul_f32 v96, v96, v16\n"
"v_mul_f32 v97, v97, v16\n"
"v_mul_f32 v98, v98, v16\n"
"v_mul_f32 v99, v99, v16\n"
"v_mul_f32 v100, v100, v16\n"
"v_mul_f32 v101, v101, v16\n"
"v_cvt_pk_bf16_f32 v18, v90, v91\n"
"v_cvt_pk_bf16_f32 v19, v92, v93\n"
"v_cvt_pk_bf16_f32 v20, v94, v95\n"
"v_cvt_pk_bf16_f32 v21, v96, v97\n"
"v_cvt_pk_bf16_f32 v22, v98, v99\n"
"v_cvt_pk_bf16_f32 v23, v100, v101\n"
"global_store_dwordx4 v0, v[18:21], s[36:37] sc1\n"
"s_mov_b64 exec, s[40:41]\n"
"global_store_dwordx2 v8, v[22:23], s[36:37] sc1\n"
"s_mov_b64 exec, -1\n"
"s_branch .La_nxt_14\n"
".La_nxt_14:\n"
"s_nop 7\n"
"s_add_u32 s56, s56, 0x8000\n"
"s_addc_u32 s57, s57, 0\n"
"s_mov_b64 s[30:31], s[56:57]\n"
"s_add_u32 s34, s34, 0x10000\n"
"s_addc_u32 s35, s35, 0\n"
"s_add_u32 s36, s36, 0x28000\n"
"s_addc_u32 s37, s37, 0\n"
"s_add_u32 s25, s25, 2\n"
"s_cmp_lt_u32 s25, 64\n"
"s_cbranch_scc1 .La_top_3\n"
"s_sub_u32 s27, s22, 1\n"
"s_cmp_ge_u32 s27, 2\n"
"s_cbranch_scc1 .La_end\n"
"s_waitcnt vmcnt(0)\n"
"s_sub_u32 s25, s25, 2\n"
"s_mov_b64 exec, 1\n"
"s_add_u32 s27, s25, 1\n"
"v_mov_b32 v100, s27\n"
"v_mov_b32 v101, 0\n"
"global_store_dword v101, v100, s[38:39] sc1\n"
"s_mov_b64 exec, -1\n"
"s_branch .La_end\n"
".La_mfail_10:\n"
"s_waitcnt vmcnt(0) lgkmcnt(0)\n"
"s_nop 7\n"
"s_nop 7\n"
"v_mov_b64 v[50:51], 0\n"
"v_mov_b64 v[52:53], 0\n"
"v_mov_b64 v[54:55], 0\n"
"v_mov_b64 v[56:57], 0\n"
"v_mov_b64 v[58:59], 0\n"
"v_mov_b64 v[60:61], 0\n"
"v_mov_b64 v[62:63], 0\n"
"v_mov_b64 v[64:65], 0\n"
"v_mov_b64 v[66:67], 0\n"
"v_mov_b64 v[68:69], 0\n"
"v_mov_b64 v[70:71], 0\n"
"v_mov_b64 v[72:73], 0\n"
"s_add_u32 s28, s30, 0x4000\n"
"s_addc_u32 s29, s31, 0\n"
"s_add_u32 s30, s30, 0x800\n"
"s_addc_u32 s31, s31, 0\n"
".La_mfl_18:\n"
"global_load_dwordx4 v[10:13], v0, s[30:31] offset:-2048 sc1\n"
"global_load_dwordx4 v[14:17], v0, s[30:31] offset:-1024 sc1\n"
"global_load_dwordx4 v[18:21], v0, s[30:31] offset:0 sc1\n"
"global_load_dwordx4 v[22:25], v0, s[30:31] offset:1024 sc1\n"
"global_load_dwordx4 v[26:29], v0, s[30:31] offset:2048 sc1\n"
"global_load_dwordx4 v[30:33], v0, s[30:31] offset:3072 sc1\n"
"global_load_dwordx4 v[34:37], v0, s[28:29] offset:0 sc1\n"
"global_load_dwordx4 v[38:41], v0, s[28:29] offset:1024 sc1\n"
"global_load_dwordx4 v[42:45], v0, s[28:29] offset:2048 sc1\n"
"global_load_dwordx4 v[46:49], v0, s[28:29] offset:3072 sc1\n"
"s_waitcnt vmcnt(9)\n"
"v_max_u32 v102, v10, v12\n"
"s_waitcnt vmcnt(8)\n"
"v_max3_u32 v102, v102, v14, v16\n"
"s_waitcnt vmcnt(7)\n"
"v_max3_u32 v102, v102, v18, v20\n"
"s_waitcnt vmcnt(6)\n"
"v_max3_u32 v102, v102, v22, v24\n"
"s_waitcnt vmcnt(5)\n"
"v_max3_u32 v102, v102, v26, v28\n"
"s_waitcnt vmcnt(4)\n"
"v_max3_u32 v102, v102, v30, v32\n"
"s_waitcnt vmcnt(3)\n"
"v_max3_u32 v102, v102, v34, v36\n"
"s_waitcnt vmcnt(2)\n"
"v_max3_u32 v102, v102, v38, v40\n"
"s_waitcnt vmcnt(1)\n"
"v_max3_u32 v102, v102, v42, v44\n"
"s_waitcnt vmcnt(0)\n"
"v_max3_u32 v102, v102, v46, v48\n"
"v_cmp_eq_u32 vcc, -1, v102\n"
"s_cbranch_vccz .La_mfo_19\n"
"s_add_u32 s26, s26, 1\n"
"s_cmp_lt_u32 s26, 0x80000\n"
"s_cbranch_scc1 .La_mfl_18\n"
"s_branch .La_abort\n"
".La_mfo_19:\n"
"v_smfmac_f32_16x16x64_bf16 v[50:53], v[10:13], a[0:7], v1\n"
"v_smfmac_f32_16x16x64_bf16 v[54:57], v[10:13], a[8:15], v1\n"
"v_smfmac_f32_16x16x64_bf16 v[58:61], v[10:13], a[16:23], v1\n"
"v_smfmac_f32_16x16x64_bf16 v[62:65], v[10:13], a[24:31], v1\n"
"v_smfmac_f32_16x16x64_bf16 v[66:69], v[10:13], a[32:39], v1\n"
"v_smfmac_f32_16x16x64_bf16 v[70:73], v[10:13], a[40:47], v1\n"
"v_smfmac_f32_16x16x64_bf16 v[50:53], v[14:17], a[48:55], v1\n"
"v_smfmac_f32_16x16x64_bf16 v[54:57], v[14:17], a[56:63], v1\n"
"v_smfmac_f32_16x16x64_bf16 v[58:61], v[14:17], a[64:71], v1\n"
"v_smfmac_f32_16x16x64_bf16 v[62:65], v[14:17], a[72:79], v1\n"
"v_smfmac_f32_16x16x64_bf16 v[66:69], v[14:17], a[80:87], v1\n"
"v_smfmac_f32_16x16x64_bf16 v[70:73], v[14:17], a[88:95], v1\n"
"v_smfmac_f32_16x16x64_bf16 v[50:53], v[18:21], a[96:103], v1\n"
"ds_read_b128 v[10:13], v4 offset:0\n"
"ds_read_b128 v[14:17], v4 offset:1024\n"
"v_smfmac_f32_16x16x64_bf16 v[54:57], v[18:21], a[104:111], v1\n"
"v_smfmac_f32_16x16x64_bf16 v[58:61], v[18:21], a[112:119], v1\n"
"v_smfmac_f32_16x16x64_bf16 v[62:65], v[18:21], a[120:127], v1\n"
"v_smfmac_f32_16x16x64_bf16 v[66:69], v[18:21], a[128:135], v1\n"
"v_smfmac_f32_16x16x64_bf16 v[70:73], v[18:21], a[136:143], v1\n"
"v_smfmac_f32_16x16x64_bf16 v[50:53], v[22:25], a[144:151], v1\n"
"v_smfmac_f32_16x16x64_bf16 v[54:57], v[22:25], a[152:159], v1\n"
"v_smfmac_f32_16x16x64_bf16 v[58:61], v[22:25], a[160:167], v1\n"
"v_smfmac_f32_16x16x64_bf16 v[62:65], v[22:25], a[168:175], v1\n"
"v_smfmac_f32_16x16x64_bf16 v[66:69], v[22:25], a[176:183], v1\n"
"v_smfmac_f32_16x16x64_bf16 v[70:73], v[22:25], a[184:191], v1\n"
"v_smfmac_f32_16x16x64_bf16 v[50:53], v[26:29], a[192:199], v1\n"
"ds_read_b128 v[18:21], v4 offset:6144\n"
"ds_read_b128 v[22:25], v4 offset:7168\n"
"v_smfmac_f32_16x16x64_bf16 v[54:57], v[26:29], a[200:207], v1\n"
"v_smfmac_f32_16x16x64_bf16 v[58:61], v[26:29], a[208:215], v1\n"
"v_smfmac_f32_16x16x64_bf16 v[62:65], v[26:29], a[216:223], v1\n"
"v_smfmac_f32_16x16x64_bf16 v[66:69], v[26:29], a[224:231], v1\n"
"v_smfmac_f32_16x16x64_bf16 v[70:73], v[26:29], a[232:239], v1\n"
"v_smfmac_f32_16x16x64_bf16 v[50:53], v[30:33], a[240:247], v1\n"
"v_smfmac_f32_16x16x64_bf16 v[54:57], v[30:33], a[248:255], v1\n"
"v_smfmac_f32_16x16x64_bf16 v[58:61], v[30:33], v[104:111], v1\n"
"v_smfmac_f32_16x16x64_bf16 v[62:65], v[30:33], v[112:119], v1\n"
"v_smfmac_f32_16x16x64_bf16 v[66:69], v[30:33], v[120:127], v1\n"
"v_smfmac_f32_16x16x64_bf16 v[70:73], v[30:33], v[128:135], v1\n"
"s_waitcnt lgkmcnt(0)\n"
"s_branch .La_mtail_11\n"
"s_branch .La_end\n"
".La_abort:\n"
"s_mov_b64 exec, -1\n"
"s_add_u32 s28, s20, 0x617000\n"
"s_addc_u32 s29, s21, 0\n"
"v_mov_b32 v10, 0\n"
"v_mov_b32 v11, s25\n"
"v_or_b32 v11, 0x10000, v11\n"
"global_store_dword v10, v11, s[28:29] sc1\n"
".La_end:\n"
"s_waitcnt vmcnt(0) lgkmcnt(0)\n"
"s_branch .Lrole_end\n"
".Lrole_v:\n"
"s_mov_b64 s[20:21], %2\n"
"s_and_b32 s23, %3, 0xff\n"
"s_bfe_u32 s24, %3, 0x10008\n"
"s_bfe_u32 s22, %3, 0x2000a\n"
"s_bfe_u32 s68, %3, 0x3000c\n"
"s_mov_b32 s26, 0\n"
"s_mov_b32 s25, s24\n"
"s_mov_b64 exec, -1\n"
"s_mov_b32 s40, 0xffffffff\n"
"s_mov_b32 s41, 0x0\n"
"s_mov_b32 s42, 0xff00ff00\n"
"s_mov_b32 s43, 0xff00ff00\n"
"s_mov_b32 s44, 0x55555555\n"
"s_mov_b32 s45, 0x55555555\n"
"v_mbcnt_lo_u32_b32 v10, -1, 0\n"
"v_mbcnt_hi_u32_b32 v10, -1, v10\n"
"v_lshlrev_b32 v0, 4, v10\n"
"v_mov_b32 v11, 0x4444\n"
"v_mov_b32 v12, 0xeeee\n"
"v_cndmask_b32 v1, v12, v11, s[44:45]\n"
"s_getpc_b64 s[28:29]\n"
".Lv_pc:\n"
"s_and_b32 s28, s28, 0xffffff80\n"
"v_lshlrev_b32 v101, 3, v0\n"
"s_lshl_b32 s27, s22, 13\n"
"v_add_u32 v101, s27, v101\n"
"v_min_u32 v101, .Lv_end-.Lv_pc-4, v101\n"
"global_load_dword v101, v101, s[28:29]\n"
"s_mul_i32 s27, s23, 384\n"
"s_mov_b64 s[58:59], %0\n"
"s_add_u32 s58, s58, s27\n"
"s_addc_u32 s59, s59, 0\n"
"s_mul_i32 s27, s23, 245760\n"
"s_mov_b64 s[64:65], %1\n"
"s_add_u32 s64, s64, s27\n"
"s_addc_u32 s65, s65, 0\n"
"s_mov_b32 s27, s22\n"
"s_mul_i32 s28, s27, 384\n"
"s_add_u32 s28, s28, 0\n"
"v_add_u32 v11, s28, v10\n"
"v_mul_u32_u24 v12, 0xaaab, v11\n"
"v_lshrrev_b32 v12, 20, v12\n"
"v_mul_u32_u24 v13, 24, v12\n"
"v_sub_u32 v13, v11, v13\n"
"v_and_b32 v14, 2, v12\n"
"v_lshrrev_b32 v15, 1, v12\n"
"v_and_b32 v15, 4, v15\n"
"v_or_b32 v14, v14, v15\n"
"v_xor_b32 v13, v13, v14\n"
"v_mul_u32_u24 v12, 0x3000, v12\n"
"v_lshl_add_u32 v84, v13, 4, v12\n"
"s_mul_i32 s28, s27, 384\n"
"s_add_u32 s28, s28, 64\n"
"v_add_u32 v11, s28, v10\n"
"v_mul_u32_u24 v12, 0xaaab, v11\n"
"v_lshrrev_b32 v12, 20, v12\n"
"v_mul_u32_u24 v13, 24, v12\n"
"v_sub_u32 v13, v11, v13\n"
"v_and_b32 v14, 2, v12\n"
"v_lshrrev_b32 v15, 1, v12\n"
"v_and_b32 v15, 4, v15\n"
"v_or_b32 v14, v14, v15\n"
"v_xor_b32 v13, v13, v14\n"
"v_mul_u32_u24 v12, 0x3000, v12\n"
"v_lshl_add_u32 v85, v13, 4, v12\n"
"s_mul_i32 s28, s27, 384\n"
"s_add_u32 s28, s28, 128\n"
"v_add_u32 v11, s28, v10\n"
"v_mul_u32_u24 v12, 0xaaab, v11\n"
"v_lshrrev_b32 v12, 20, v12\n"
"v_mul_u32_u24 v13, 24, v12\n"
"v_sub_u32 v13, v11, v13\n"
"v_and_b32 v14, 2, v12\n"
"v_lshrrev_b32 v15, 1, v12\n"
"v_and_b32 v15, 4, v15\n"
"v_or_b32 v14, v14, v15\n"
"v_xor_b32 v13, v13, v14\n"
"v_mul_u32_u24 v12, 0x3000, v12\n"
"v_lshl_add_u32 v86, v13, 4, v12\n"
"s_mul_i32 s28, s27, 384\n"
"s_add_u32 s28, s28, 192\n"
"v_add_u32 v11, s28, v10\n"
"v_mul_u32_u24 v12, 0xaaab, v11\n"
"v_lshrrev_b32 v12, 20, v12\n"
"v_mul_u32_u24 v13, 24, v12\n"
"v_sub_u32 v13, v11, v13\n"
"v_and_b32 v14, 2, v12\n"
"v_lshrrev_b32 v15, 1, v12\n"
"v_and_b32 v15, 4, v15\n"
"v_or_b32 v14, v14, v15\n"
"v_xor_b32 v13, v13, v14\n"
"v_mul_u32_u24 v12, 0x3000, v12\n"
"v_lshl_add_u32 v87, v13, 4, v12\n"
"s_mul_i32 s28, s27, 384\n"
"s_add_u32 s28, s28, 256\n"
"v_add_u32 v11, s28, v10\n"
"v_mul_u32_u24 v12, 0xaaab, v11\n"
"v_lshrrev_b32 v12, 20, v12\n"
"v_mul_u32_u24 v13, 24, v12\n"
"v_sub_u32 v13, v11, v13\n"
"v_and_b32 v14, 2, v12\n"
"v_lshrrev_b32 v15, 1, v12\n"
"v_and_b32 v15, 4, v15\n"
"v_or_b32 v14, v14, v15\n"
"v_xor_b32 v13, v13, v14\n"
"v_mul_u32_u24 v12, 0x3000, v12\n"
"v_lshl_add_u32 v88, v13, 4, v12\n"
"s_mul_i32 s28, s27, 384\n"
"s_add_u32 s28, s28, 320\n"
"v_add_u32 v11, s28, v10\n"
"v_mul_u32_u24 v12, 0xaaab, v11\n"
"v_lshrrev_b32 v12, 20, v12\n"
"v_mul_u32_u24 v13, 24, v12\n"
"v_sub_u32 v13, v11, v13\n"
"v_and_b32 v14, 2, v12\n"
"v_lshrrev_b32 v15, 1, v12\n"
"v_and_b32 v15, 4, v15\n"
"v_or_b32 v14, v14, v15\n"
"v_xor_b32 v13, v13, v14\n"
"v_mul_u32_u24 v12, 0x3000, v12\n"
"v_lshl_add_u32 v89, v13, 4, v12\n"
"v_lshrrev_b32 v11, 4, v10\n"
"v_lshlrev_b32 v11, 3, v11\n"
"v_bfe_u32 v12, v10, 2, 2\n"
"v_add_u32 v11, v11, v12\n"
"v_and_b32 v14, 2, v11\n"
"v_lshrrev_b32 v15, 1, v11\n"
"v_and_b32 v15, 4, v15\n"
"v_or_b32 v14, v14, v15\n"
"v_mul_u32_u24 v11, 384, v11\n"
"v_and_b32 v12, 1, v10\n"
"v_lshl_add_u32 v11, v12, 3, v11\n"
"v_bfe_u32 v12, v10, 1, 1\n"
"s_mul_i32 s28, s27, 6\n"
"s_add_u32 s28, s28, 0\n"
"v_add_u32 v13, s28, v12\n"
"v_xor_b32 v13, v13, v14\n"
"v_lshl_add_u32 v90, v13, 4, v11\n"
"s_mul_i32 s28, s27, 6\n"
"s_add_u32 s28, s28, 2\n"
"v_add_u32 v13, s28, v12\n"
"v_xor_b32 v13, v13, v14\n"
"v_lshl_add_u32 v91, v13, 4, v11\n"
"s_mul_i32 s28, s27, 6\n"
"s_add_u32 s28, s28, 4\n"
"v_add_u32 v13, s28, v12\n"
"v_xor_b32 v13, v13, v14\n"
"v_lshl_add_u32 v92, v13, 4, v11\n"
"s_mul_i32 s28, s22, 320\n"
"s_add_u32 s28, s28, 0\n"
"v_add_u32 v11, s28, v10\n"
"v_mul_u32_u24 v12, 52429, v11\n"
"v_lshrrev_b32 v12, 22, v12\n"
"v_mul_u32_u24 v13, 80, v12\n"
"v_sub_u32 v13, v11, v13\n"
"v_and_b32 v14, 7, v12\n"
"v_lshlrev_b32 v14, 1, v14\n"
"v_xor_b32 v13, v13, v14\n"
"v_mul_u32_u24 v12, 1280, v12\n"
"v_lshl_add_u32 v93, v13, 4, v12\n"
"s_mul_i32 s28, s22, 320\n"
"s_add_u32 s28, s28, 64\n"
"v_add_u32 v11, s28, v10\n"
"v_mul_u32_u24 v12, 52429, v11\n"
"v_lshrrev_b32 v12, 22, v12\n"
"v_mul_u32_u24 v13, 80, v12\n"
"v_sub_u32 v13, v11, v13\n"
"v_and_b32 v14, 7, v12\n"
"v_lshlrev_b32 v14, 1, v14\n"
"v_xor_b32 v13, v13, v14\n"
"v_mul_u32_u24 v12, 1280, v12\n"
"v_lshl_add_u32 v94, v13, 4, v12\n"
"s_mul_i32 s28, s22, 320\n"
"s_add_u32 s28, s28, 128\n"
"v_add_u32 v11, s28, v10\n"
"v_mul_u32_u24 v12, 52429, v11\n"
"v_lshrrev_b32 v12, 22, v12\n"
"v_mul_u32_u24 v13, 80, v12\n"
"v_sub_u32 v13, v11, v13\n"
"v_and_b32 v14, 7, v12\n"
"v_lshlrev_b32 v14, 1, v14\n"
"v_xor_b32 v13, v13, v14\n"
"v_mul_u32_u24 v12, 1280, v12\n"
"v_lshl_add_u32 v95, v13, 4, v12\n"
"s_mul_i32 s28, s22, 320\n"
"s_add_u32 s28, s28, 192\n"
"v_add_u32 v11, s28, v10\n"
"v_mul_u32_u24 v12, 52429, v11\n"
"v_lshrrev_b32 v12, 22, v12\n"
"v_mul_u32_u24 v13, 80, v12\n"
"v_sub_u32 v13, v11, v13\n"
"v_and_b32 v14, 7, v12\n"
"v_lshlrev_b32 v14, 1, v14\n"
"v_xor_b32 v13, v13, v14\n"
"v_mul_u32_u24 v12, 1280, v12\n"
"v_lshl_add_u32 v96, v13, 4, v12\n"
"s_mul_i32 s28, s22, 320\n"
"s_add_u32 s28, s28, 256\n"
"v_add_u32 v11, s28, v10\n"
"v_mul_u32_u24 v12, 52429, v11\n"
"v_lshrrev_b32 v12, 22, v12\n"
"v_mul_u32_u24 v13, 80, v12\n"
"v_sub_u32 v13, v11, v13\n"
"v_and_b32 v14, 7, v12\n"
"v_lshlrev_b32 v14, 1, v14\n"
"v_xor_b32 v13, v13, v14\n"
"v_mul_u32_u24 v12, 1280, v12\n"
"v_lshl_add_u32 v97, v13, 4, v12\n"
"v_bfe_u32 v11, v10, 2, 2\n"
"v_lshlrev_b32 v11, 6, v11\n"
"v_and_b32 v12, 1, v10\n"
"v_lshl_add_u32 v11, v12, 5, v11\n"
"v_lshrrev_b32 v12, 4, v10\n"
"v_lshl_add_u32 v11, v12, 3, v11\n"
"v_bfe_u32 v12, v10, 1, 1\n"
"v_lshl_add_u32 v11, v12, 1, v11\n"
"s_mul_i32 s27, s22, 768\n"
"s_add_u32 s27, s27, 137216\n"
"v_add_u32 v2, s27, v11\n"
"v_lshrrev_b32 v11, 5, v10\n"
"v_lshlrev_b32 v11, 4, v11\n"
"v_bfe_u32 v12, v10, 3, 1\n"
"v_lshl_add_u32 v11, v12, 3, v11\n"
"v_bfe_u32 v12, v10, 4, 1\n"
"v_lshl_add_u32 v11, v12, 2, v11\n"
"v_bfe_u32 v12, v10, 2, 1\n"
"v_add_u32 v11, v11, v12\n"
"v_lshlrev_b32 v11, 5, v11\n"
"v_and_b32 v12, 3, v10\n"
"v_lshl_add_u32 v11, v12, 3, v11\n"
"v_add_u32 v3, 0x21800, v11\n"
"s_mul_i32 s27, s22, 18432\n"
"v_add_u32 v4, s27, v0\n"
"v_add_u32 v5, 0x12000, v0\n"
"v_add_u32 v6, 0x1f000, v0\n"
"v_and_b32 v11, 15, v10\n"
"v_lshlrev_b32 v11, 4, v11\n"
"v_lshrrev_b32 v12, 4, v10\n"
"v_lshl_add_u32 v7, v12, 14, v11\n"
"v_mov_b32 v99, v10\n"
"s_lshl_b32 s27, s24, 5\n"
"s_add_u32 s27, s27, s23\n"
"s_add_u32 s27, s27, 64\n"
"s_lshl_b32 s27, s27, 2\n"
"s_add_u32 s27, s27, s22\n"
"s_lshl_b32 s27, s27, 2\n"
"s_add_u32 s28, s27, 0x622000\n"
"s_add_u32 s90, s20, s28\n"
"s_addc_u32 s91, s21, 0\n"
"s_lshl_b32 s27, s27, 1\n"
"s_add_u32 s27, s27, 0x623000\n"
"s_add_u32 s92, s20, s27\n"
"s_addc_u32 s93, s21, 0\n"
"v_mov_b32 v9, 0\n"
"global_load_dword v10, v9, s[90:91] sc1\n"
"global_load_dword v11, v9, s[92:93] sc1\n"
"s_mul_i32 s27, s22, 6144\n"
"s_add_u32 s27, s27, 0\n"
"s_mov_b32 m0, s27\n"
"s_add_u32 s27, s27, 1024\n"
"global_load_lds_dwordx4 v84, s[58:59]\n"
"s_mov_b32 m0, s27\n"
"s_add_u32 s27, s27, 1024\n"
"global_load_lds_dwordx4 v85, s[58:59]\n"
"s_mov_b32 m0, s27\n"
"s_add_u32 s27, s27, 1024\n"
"global_load_lds_dwordx4 v86, s[58:59]\n"
"s_mov_b32 m0, s27\n"
"s_add_u32 s27, s27, 1024\n"
"global_load_lds_dwordx4 v87, s[58:59]\n"
"s_mov_b32 m0, s27\n"
"s_add_u32 s27, s27, 1024\n"
"global_load_lds_dwordx4 v88, s[58:59]\n"
"s_mov_b32 m0, s27\n"
"s_nop 0\n"
"global_load_lds_dwordx4 v89, s[58:59]\n"
"s_add_u32 s58, s58, 0xc0000\n"
"s_addc_u32 s59, s59, 0\n"
"s_mul_i32 s27, s22, 6144\n"
"s_add_u32 s27, s27, 24576\n"
"s_mov_b32 m0, s27\n"
"s_add_u32 s27, s27, 1024\n"
"global_load_lds_dwordx4 v84, s[58:59]\n"
"s_mov_b32 m0, s27\n"
"s_add_u32 s27, s27, 1024\n"
"global_load_lds_dwordx4 v85, s[58:59]\n"
"s_mov_b32 m0, s27\n"
"s_add_u32 s27, s27, 1024\n"
"global_load_lds_dwordx4 v86, s[58:59]\n"
"s_mov_b32 m0, s27\n"
"s_add_u32 s27, s27, 1024\n"
"global_load_lds_dwordx4 v87, s[58:59]\n"
"s_mov_b32 m0, s27\n"
"s_add_u32 s27, s27, 1024\n"
"global_load_lds_dwordx4 v88, s[58:59]\n"
"s_mov_b32 m0, s27\n"
"s_nop 0\n"
"global_load_lds_dwordx4 v89, s[58:59]\n"
"s_add_u32 s58, s58, 0xc0000\n"
"s_addc_u32 s59, s59, 0\n"
"s_mul_i32 s27, s22, 6144\n"
"s_add_u32 s27, s27, 49152\n"
"s_mov_b32 m0, s27\n"
"s_add_u32 s27, s27, 1024\n"
"global_load_lds_dwordx4 v84, s[58:59]\n"
"s_mov_b32 m0, s27\n"
"s_add_u32 s27, s27, 1024\n"
"global_load_lds_dwordx4 v85, s[58:59]\n"
"s_mov_b32 m0, s27\n"
"s_add_u32 s27, s27, 1024\n"
"global_load_lds_dwordx4 v86, s[58:59]\n"
"s_mov_b32 m0, s27\n"
"s_add_u32 s27, s27, 1024\n"
"global_load_lds_dwordx4 v87, s[58:59]\n"
"s_mov_b32 m0, s27\n"
"s_add_u32 s27, s27, 1024\n"
"global_load_lds_dwordx4 v88, s[58:59]\n"
"s_mov_b32 m0, s27\n"
"s_nop 0\n"
"global_load_lds_dwordx4 v89, s[58:59]\n"
"s_add_u32 s58, s58, 0xc0000\n"
"s_addc_u32 s59, s59, 0\n"
"s_mul_i32 s27, s22, 6144\n"
"s_add_u32 s27, s27, 114688\n"
"s_mov_b32 m0, s27\n"
"s_add_u32 s27, s27, 1024\n"
"global_load_lds_dwordx4 v84, s[58:59]\n"
"s_mov_b32 m0, s27\n"
"s_add_u32 s27, s27, 1024\n"
"global_load_lds_dwordx4 v85, s[58:59]\n"
"s_mov_b32 m0, s27\n"
"s_add_u32 s27, s27, 1024\n"
"global_load_lds_dwordx4 v86, s[58:59]\n"
"s_mov_b32 m0, s27\n"
"s_add_u32 s27, s27, 1024\n"
"global_load_lds_dwordx4 v87, s[58:59]\n"
"s_mov_b32 m0, s27\n"
"s_add_u32 s27, s27, 1024\n"
"global_load_lds_dwordx4 v88, s[58:59]\n"
"s_mov_b32 m0, s27\n"
"s_nop 0\n"
"global_load_lds_dwordx4 v89, s[58:59]\n"
"s_add_u32 s58, s58, 0xc0000\n"
"s_addc_u32 s59, s59, 0\n"
"s_waitcnt vmcnt(18)\n"
"s_waitcnt lgkmcnt(0)\n"
"s_barrier\n"
"v_add_u32 v8, 1, v10\n"
"v_add_u32 v10, 2, v10\n"
"v_cmp_eq_u32 vcc, v8, v11\n"
"s_nop 1\n"
"v_cndmask_b32 v8, v8, v10, vcc\n"
"s_mov_b64 exec, 1\n"
"global_store_dword v9, v8, s[90:91] sc1\n"
"s_mov_b64 exec, -1\n"
"s_mul_i32 s27, s22, 6144\n"
"s_add_u32 s27, s27, 139264\n"
"s_mov_b32 m0, s27\n"
"s_add_u32 s27, s27, 1024\n"
"global_load_lds_dwordx4 v84, s[58:59]\n"
"s_mov_b32 m0, s27\n"
"s_add_u32 s27, s27, 1024\n"
"global_load_lds_dwordx4 v85, s[58:59]\n"
"s_mov_b32 m0, s27\n"
"s_add_u32 s27, s27, 1024\n"
"global_load_lds_dwordx4 v86, s[58:59]\n"
"s_mov_b32 m0, s27\n"
"s_add_u32 s27, s27, 1024\n"
"global_load_lds_dwordx4 v87, s[58:59]\n"
"s_mov_b32 m0, s27\n"
"s_add_u32 s27, s27, 1024\n"
"global_load_lds_dwordx4 v88, s[58:59]\n"
"s_mov_b32 m0, s27\n"
"s_nop 0\n"
"global_load_lds_dwordx4 v89, s[58:59]\n"
"s_add_u32 s58, s58, 0xc0000\n"
"s_addc_u32 s59, s59, 0\n"
"ds_read_b64_tr_b16 a[0:1], v90 offset:0\n"
"ds_read_b64_tr_b16 a[2:3], v90 offset:1536\n"
"ds_read_b64_tr_b16 a[8:9], v91 offset:0\n"
"ds_read_b64_tr_b16 a[10:11], v91 offset:1536\n"
"ds_read_b64_tr_b16 a[16:17], v92 offset:0\n"
"ds_read_b64_tr_b16 a[18:19], v92 offset:1536\n"
"ds_read_b64_tr_b16 a[4:5], v90 offset:12288\n"
"ds_read_b64_tr_b16 a[6:7], v90 offset:13824\n"
"ds_read_b64_tr_b16 a[12:13], v91 offset:12288\n"
"ds_read_b64_tr_b16 a[14:15], v91 offset:13824\n"
"ds_read_b64_tr_b16 a[20:21], v92 offset:12288\n"
"ds_read_b64_tr_b16 a[22:23], v92 offset:13824\n"
"s_waitcnt vmcnt(18)\n"
"s_waitcnt lgkmcnt(0)\n"
"s_barrier\n"
"s_mul_i32 s27, s22, 6144\n"
"s_add_u32 s27, s27, 0\n"
"s_mov_b32 m0, s27\n"
"s_add_u32 s27, s27, 1024\n"
"global_load_lds_dwordx4 v84, s[58:59]\n"
"s_mov_b32 m0, s27\n"
"s_add_u32 s27, s27, 1024\n"
"global_load_lds_dwordx4 v85, s[58:59]\n"
"s_mov_b32 m0, s27\n"
"s_add_u32 s27, s27, 1024\n"
"global_load_lds_dwordx4 v86, s[58:59]\n"
"s_mov_b32 m0, s27\n"
"s_add_u32 s27, s27, 1024\n"
"global_load_lds_dwordx4 v87, s[58:59]\n"
"s_mov_b32 m0, s27\n"
"s_add_u32 s27, s27, 1024\n"
"global_load_lds_dwordx4 v88, s[58:59]\n"
"s_mov_b32 m0, s27\n"
"s_nop 0\n"
"global_load_lds_dwordx4 v89, s[58:59]\n"
"s_add_u32 s58, s58, 0xc0000\n"
"s_addc_u32 s59, s59, 0\n"
"ds_read_b64_tr_b16 a[24:25], v90 offset:24576\n"
"ds_read_b64_tr_b16 a[26:27], v90 offset:26112\n"
"ds_read_b64_tr_b16 a[32:33], v91 offset:24576\n"
"ds_read_b64_tr_b16 a[34:35], v91 offset:26112\n"
"ds_read_b64_tr_b16 a[40:41], v92 offset:24576\n"
"ds_read_b64_tr_b16 a[42:43], v92 offset:26112\n"
"ds_read_b64_tr_b16 a[28:29], v90 offset:36864\n"
"ds_read_b64_tr_b16 a[30:31], v90 offset:38400\n"
"ds_read_b64_tr_b16 a[36:37], v91 offset:36864\n"
"ds_read_b64_tr_b16 a[38:39], v91 offset:38400\n"
"ds_read_b64_tr_b16 a[44:45], v92 offset:36864\n"
"ds_read_b64_tr_b16 a[46:47], v92 offset:38400\n"
"s_waitcnt vmcnt(18)\n"
"s_waitcnt lgkmcnt(0)\n"
"s_barrier\n"
"s_mul_i32 s27, s22, 6144\n"
"s_add_u32 s27, s27, 24576\n"
"s_mov_b32 m0, s27\n"
"s_add_u32 s27, s27, 1024\n"
"global_load_lds_dwordx4 v84, s[58:59]\n"
"s_mov_b32 m0, s27\n"
"s_add_u32 s27, s27, 1024\n"
"global_load_lds_dwordx4 v85, s[58:59]\n"
"s_mov_b32 m0, s27\n"
"s_add_u32 s27, s27, 1024\n"
"global_load_lds_dwordx4 v86, s[58:59]\n"
"s_mov_b32 m0, s27\n"
"s_add_u32 s27, s27, 1024\n"
"global_load_lds_dwordx4 v87, s[58:59]\n"
"s_mov_b32 m0, s27\n"
"s_add_u32 s27, s27, 1024\n"
"global_load_lds_dwordx4 v88, s[58:59]\n"
"s_mov_b32 m0, s27\n"
"s_nop 0\n"
"global_load_lds_dwordx4 v89, s[58:59]\n"
"s_add_u32 s58, s58, 0xc0000\n"
"s_addc_u32 s59, s59, 0\n"
"ds_read_b64_tr_b16 a[48:49], v90 offset:49152\n"
"ds_read_b64_tr_b16 a[50:51], v90 offset:50688\n"
"ds_read_b64_tr_b16 a[56:57], v91 offset:49152\n"
"ds_read_b64_tr_b16 a[58:59], v91 offset:50688\n"
"ds_read_b64_tr_b16 a[64:65], v92 offset:49152\n"
"ds_read_b64_tr_b16 a[66:67], v92 offset:50688\n"
"ds_read_b64_tr_b16 a[52:53], v90 offset:61440\n"
"ds_read_b64_tr_b16 a[54:55], v90 offset:62976\n"
"ds_read_b64_tr_b16 a[60:61], v91 offset:61440\n"
"ds_read_b64_tr_b16 a[62:63], v91 offset:62976\n"
"ds_read_b64_tr_b16 a[68:69], v92 offset:61440\n"
"ds_read_b64_tr_b16 a[70:71], v92 offset:62976\n"
"s_waitcnt vmcnt(18)\n"
"s_waitcnt lgkmcnt(0)\n"
"s_barrier\n"
"s_mul_i32 s27, s22, 6144\n"
"s_add_u32 s27, s27, 49152\n"
"s_mov_b32 m0, s27\n"
"s_add_u32 s27, s27, 1024\n"
"global_load_lds_dwordx4 v84, s[58:59]\n"
"s_mov_b32 m0, s27\n"
"s_add_u32 s27, s27, 1024\n"
"global_load_lds_dwordx4 v85, s[58:59]\n"
"s_mov_b32 m0, s27\n"
"s_add_u32 s27, s27, 1024\n"
"global_load_lds_dwordx4 v86, s[58:59]\n"
"s_mov_b32 m0, s27\n"
"s_add_u32 s27, s27, 1024\n"
"global_load_lds_dwordx4 v87, s[58:59]\n"
"s_mov_b32 m0, s27\n"
"s_add_u32 s27, s27, 1024\n"
"global_load_lds_dwordx4 v88, s[58:59]\n"
"s_mov_b32 m0, s27\n"
"s_nop 0\n"
"global_load_lds_dwordx4 v89, s[58:59]\n"
"s_add_u32 s58, s58, 0xc0000\n"
"s_addc_u32 s59, s59, 0\n"
"v_add_u32 v90, 0x1c000, v90\n"
"v_add_u32 v91, 0x1c000, v91\n"
"v_add_u32 v92, 0x1c000, v92\n"
"ds_read_b64_tr_b16 a[72:73], v90 offset:0\n"
"ds_read_b64_tr_b16 a[74:75], v90 offset:1536\n"
"ds_read_b64_tr_b16 a[80:81], v91 offset:0\n"
"ds_read_b64_tr_b16 a[82:83], v91 offset:1536\n"
"ds_read_b64_tr_b16 a[88:89], v92 offset:0\n"
"ds_read_b64_tr_b16 a[90:91], v92 offset:1536\n"
"ds_read_b64_tr_b16 a[76:77], v90 offset:12288\n"
"ds_read_b64_tr_b16 a[78:79], v90 offset:13824\n"
"ds_read_b64_tr_b16 a[84:85], v91 offset:12288\n"
"ds_read_b64_tr_b16 a[86:87], v91 offset:13824\n"
"ds_read_b64_tr_b16 a[92:93], v92 offset:12288\n"
"ds_read_b64_tr_b16 a[94:95], v92 offset:13824\n"
"s_waitcnt vmcnt(18)\n"
"s_waitcnt lgkmcnt(0)\n"
"s_barrier\n"
"s_mul_i32 s27, s22, 6144\n"
"s_add_u32 s27, s27, 114688\n"
"s_mov_b32 m0, s27\n"
"s_add_u32 s27, s27, 1024\n"
"global_load_lds_dwordx4 v84, s[58:59]\n"
"s_mov_b32 m0, s27\n"
"s_add_u32 s27, s27, 1024\n"
"global_load_lds_dwordx4 v85, s[58:59]\n"
"s_mov_b32 m0, s27\n"
"s_add_u32 s27, s27, 1024\n"
"global_load_lds_dwordx4 v86, s[58:59]\n"
"s_mov_b32 m0, s27\n"
"s_add_u32 s27, s27, 1024\n"
"global_load_lds_dwordx4 v87, s[58:59]\n"
"s_mov_b32 m0, s27\n"
"s_add_u32 s27, s27, 1024\n"
"global_load_lds_dwordx4 v88, s[58:59]\n"
"s_mov_b32 m0, s27\n"
"s_nop 0\n"
"global_load_lds_dwordx4 v89, s[58:59]\n"
"s_add_u32 s58, s58, 0xc0000\n"
"s_addc_u32 s59, s59, 0\n"
"ds_read_b64_tr_b16 a[96:97], v90 offset:24576\n"
"ds_read_b64_tr_b16 a[98:99], v90 offset:26112\n"
"ds_read_b64_tr_b16 a[104:105], v91 offset:24576\n"
"ds_read_b64_tr_b16 a[106:107], v91 offset:26112\n"
"ds_read_b64_tr_b16 a[112:113], v92 offset:24576\n"
"ds_read_b64_tr_b16 a[114:115], v92 offset:26112\n"
"ds_read_b64_tr_b16 a[100:101], v90 offset:36864\n"
"ds_read_b64_tr_b16 a[102:103], v90 offset:38400\n"
"ds_read_b64_tr_b16 a[108:109], v91 offset:36864\n"
"ds_read_b64_tr_b16 a[110:111], v91 offset:38400\n"
"ds_read_b64_tr_b16 a[116:117], v92 offset:36864\n"
"ds_read_b64_tr_b16 a[118:119], v92 offset:38400\n"
"s_waitcnt vmcnt(18)\n"
"s_waitcnt lgkmcnt(0)\n"
"s_barrier\n"
"s_mul_i32 s27, s22, 6144\n"
"s_add_u32 s27, s27, 139264\n"
"s_mov_b32 m0, s27\n"
"s_add_u32 s27, s27, 1024\n"
"global_load_lds_dwordx4 v84, s[58:59]\n"
"s_mov_b32 m0, s27\n"
"s_add_u32 s27, s27, 1024\n"
"global_load_lds_dwordx4 v85, s[58:59]\n"
"s_mov_b32 m0, s27\n"
"s_add_u32 s27, s27, 1024\n"
"global_load_lds_dwordx4 v86, s[58:59]\n"
"s_mov_b32 m0, s27\n"
"s_add_u32 s27, s27, 1024\n"
"global_load_lds_dwordx4 v87, s[58:59]\n"
"s_mov_b32 m0, s27\n"
"s_add_u32 s27, s27, 1024\n"
"global_load_lds_dwordx4 v88, s[58:59]\n"
"s_mov_b32 m0, s27\n"
"s_nop 0\n"
"global_load_lds_dwordx4 v89, s[58:59]\n"
"s_add_u32 s58, s58, 0xc0000\n"
"s_addc_u32 s59, s59, 0\n"
"v_subrev_u32 v90, 0x1c000, v90\n"
"v_subrev_u32 v91, 0x1c000, v91\n"
"v_subrev_u32 v92, 0x1c000, v92\n"
"ds_read_b64_tr_b16 a[120:121], v90 offset:0\n"
"ds_read_b64_tr_b16 a[122:123], v90 offset:1536\n"
"ds_read_b64_tr_b16 a[128:129], v91 offset:0\n"
"ds_read_b64_tr_b16 a[130:131], v91 offset:1536\n"
"ds_read_b64_tr_b16 a[136:137], v92 offset:0\n"
"ds_read_b64_tr_b16 a[138:139], v92 offset:1536\n"
"ds_read_b64_tr_b16 a[124:125], v90 offset:12288\n"
"ds_read_b64_tr_b16 a[126:127], v90 offset:13824\n"
"ds_read_b64_tr_b16 a[132:133], v91 offset:12288\n"
"ds_read_b64_tr_b16 a[134:135], v91 offset:13824\n"
"ds_read_b64_tr_b16 a[140:141], v92 offset:12288\n"
"ds_read_b64_tr_b16 a[142:143], v92 offset:13824\n"
"s_waitcnt vmcnt(18)\n"
"s_waitcnt lgkmcnt(0)\n"
"s_barrier\n"
"s_mul_i32 s27, s22, 5120\n"
"s_add_u32 s27, s27, 0\n"
"s_mov_b32 m0, s27\n"
"s_add_u32 s27, s27, 1024\n"
"global_load_lds_dwordx4 v93, s[64:65]\n"
"s_mov_b32 m0, s27\n"
"s_add_u32 s27, s27, 1024\n"
"global_load_lds_dwordx4 v94, s[64:65]\n"
"s_mov_b32 m0, s27\n"
"s_add_u32 s27, s27, 1024\n"
"global_load_lds_dwordx4 v95, s[64:65]\n"
"s_mov_b32 m0, s27\n"
"s_add_u32 s27, s27, 1024\n"
"global_load_lds_dwordx4 v96, s[64:65]\n"
"s_mov_b32 m0, s27\n"
"s_nop 0\n"
"global_load_lds_dwordx4 v97, s[64:65]\n"
"s_add_u32 s64, s64, 0x5000\n"
"s_addc_u32 s65, s65, 0\n"
"ds_read_b64_tr_b16 a[144:145], v90 offset:24576\n"
"ds_read_b64_tr_b16 a[146:147], v90 offset:26112\n"
"ds_read_b64_tr_b16 a[152:153], v91 offset:24576\n"
"ds_read_b64_tr_b16 a[154:155], v91 offset:26112\n"
"ds_read_b64_tr_b16 a[160:161], v92 offset:24576\n"
"ds_read_b64_tr_b16 a[162:163], v92 offset:26112\n"
"ds_read_b64_tr_b16 a[148:149], v90 offset:36864\n"
"ds_read_b64_tr_b16 a[150:151], v90 offset:38400\n"
"ds_read_b64_tr_b16 a[156:157], v91 offset:36864\n"
"ds_read_b64_tr_b16 a[158:159], v91 offset:38400\n"
"ds_read_b64_tr_b16 a[164:165], v92 offset:36864\n"
"ds_read_b64_tr_b16 a[166:167], v92 offset:38400\n"
"s_waitcnt vmcnt(17)\n"
"s_waitcnt lgkmcnt(0)\n"
"s_barrier\n"
"s_mul_i32 s27, s22, 5120\n"
"s_add_u32 s27, s27, 24576\n"
"s_mov_b32 m0, s27\n"
"s_add_u32 s27, s27, 1024\n"
"global_load_lds_dwordx4 v93, s[64:65]\n"
"s_mov_b32 m0, s27\n"
"s_add_u32 s27, s27, 1024\n"
"global_load_lds_dwordx4 v94, s[64:65]\n"
"s_mov_b32 m0, s27\n"
"s_add_u32 s27, s27, 1024\n"
"global_load_lds_dwordx4 v95, s[64:65]\n"
"s_mov_b32 m0, s27\n"
"s_add_u32 s27, s27, 1024\n"
"global_load_lds_dwordx4 v96, s[64:65]\n"
"s_mov_b32 m0, s27\n"
"s_nop 0\n"
"global_load_lds_dwordx4 v97, s[64:65]\n"
"s_add_u32 s64, s64, 0x5000\n"
"s_addc_u32 s65, s65, 0\n"
"ds_read_b64_tr_b16 a[168:169], v90 offset:49152\n"
"ds_read_b64_tr_b16 a[170:171], v90 offset:50688\n"
"ds_read_b64_tr_b16 a[176:177], v91 offset:49152\n"
"ds_read_b64_tr_b16 a[178:179], v91 offset:50688\n"
"ds_read_b64_tr_b16 a[184:185], v92 offset:49152\n"
"ds_read_b64_tr_b16 a[186:187], v92 offset:50688\n"
"ds_read_b64_tr_b16 a[172:173], v90 offset:61440\n"
"ds_read_b64_tr_b16 a[174:175], v90 offset:62976\n"
"ds_read_b64_tr_b16 a[180:181], v91 offset:61440\n"
"ds_read_b64_tr_b16 a[182:183], v91 offset:62976\n"
"ds_read_b64_tr_b16 a[188:189], v92 offset:61440\n"
"ds_read_b64_tr_b16 a[190:191], v92 offset:62976\n"
"s_waitcnt vmcnt(16)\n"
"s_waitcnt lgkmcnt(0)\n"
"s_barrier\n"
"s_mul_i32 s27, s22, 5120\n"
"s_add_u32 s27, s27, 49152\n"
"s_mov_b32 m0, s27\n"
"s_add_u32 s27, s27, 1024\n"
"global_load_lds_dwordx4 v93, s[64:65]\n"
"s_mov_b32 m0, s27\n"
"s_add_u32 s27, s27, 1024\n"
"global_load_lds_dwordx4 v94, s[64:65]\n"
"s_mov_b32 m0, s27\n"
"s_add_u32 s27, s27, 1024\n"
"global_load_lds_dwordx4 v95, s[64:65]\n"
"s_mov_b32 m0, s27\n"
"s_add_u32 s27, s27, 1024\n"
"global_load_lds_dwordx4 v96, s[64:65]\n"
"s_mov_b32 m0, s27\n"
"s_nop 0\n"
"global_load_lds_dwordx4 v97, s[64:65]\n"
"s_add_u32 s64, s64, 0x5000\n"
"s_addc_u32 s65, s65, 0\n"
"v_add_u32 v90, 0x1c000, v90\n"
"v_add_u32 v91, 0x1c000, v91\n"
"v_add_u32 v92, 0x1c000, v92\n"
"ds_read_b64_tr_b16 a[192:193], v90 offset:0\n"
"ds_read_b64_tr_b16 a[194:195], v90 offset:1536\n"
"ds_read_b64_tr_b16 a[200:201], v91 offset:0\n"
"ds_read_b64_tr_b16 a[202:203], v91 offset:1536\n"
"ds_read_b64_tr_b16 a[208:209], v92 offset:0\n"
"ds_read_b64_tr_b16 a[210:211], v92 offset:1536\n"
"ds_read_b64_tr_b16 a[196:197], v90 offset:12288\n"
"ds_read_b64_tr_b16 a[198:199], v90 offset:13824\n"
"ds_read_b64_tr_b16 a[204:205], v91 offset:12288\n"
"ds_read_b64_tr_b16 a[206:207], v91 offset:13824\n"
"ds_read_b64_tr_b16 a[212:213], v92 offset:12288\n"
"ds_read_b64_tr_b16 a[214:215], v92 offset:13824\n"
"s_waitcnt vmcnt(15)\n"
"s_waitcnt lgkmcnt(0)\n"
"s_barrier\n"
"s_mul_i32 s27, s22, 5120\n"
"s_add_u32 s27, s27, 114688\n"
"s_mov_b32 m0, s27\n"
"s_add_u32 s27, s27, 1024\n"
"global_load_lds_dwordx4 v93, s[64:65]\n"
"s_mov_b32 m0, s27\n"
"s_add_u32 s27, s27, 1024\n"
"global_load_lds_dwordx4 v94, s[64:65]\n"
"s_mov_b32 m0, s27\n"
"s_add_u32 s27, s27, 1024\n"
"global_load_lds_dwordx4 v95, s[64:65]\n"
"s_mov_b32 m0, s27\n"
"s_add_u32 s27, s27, 1024\n"
"global_load_lds_dwordx4 v96, s[64:65]\n"
"s_mov_b32 m0, s27\n"
"s_nop 0\n"
"global_load_lds_dwordx4 v97, s[64:65]\n"
"s_add_u32 s64, s64, 0x5000\n"
"s_addc_u32 s65, s65, 0\n"
"ds_read_b64_tr_b16 a[216:217], v90 offset:24576\n"
"ds_read_b64_tr_b16 a[218:219], v90 offset:26112\n"
"ds_read_b64_tr_b16 a[224:225], v91 offset:24576\n"
"ds_read_b64_tr_b16 a[226:227], v91 offset:26112\n"
"ds_read_b64_tr_b16 a[232:233], v92 offset:24576\n"
"ds_read_b64_tr_b16 a[234:235], v92 offset:26112\n"
"ds_read_b64_tr_b16 a[220:221], v90 offset:36864\n"
"ds_read_b64_tr_b16 a[222:223], v90 offset:38400\n"
"ds_read_b64_tr_b16 a[228:229], v91 offset:36864\n"
"ds_read_b64_tr_b16 a[230:231], v91 offset:38400\n"
"ds_read_b64_tr_b16 a[236:237], v92 offset:36864\n"
"ds_read_b64_tr_b16 a[238:239], v92 offset:38400\n"
"s_waitcnt vmcnt(15)\n"
"s_waitcnt lgkmcnt(0)\n"
"s_barrier\n"
"s_mul_i32 s27, s22, 5120\n"
"s_add_u32 s27, s27, 139264\n"
"s_mov_b32 m0, s27\n"
"s_add_u32 s27, s27, 1024\n"
"global_load_lds_dwordx4 v93, s[64:65]\n"
"s_mov_b32 m0, s27\n"
"s_add_u32 s27, s27, 1024\n"
"global_load_lds_dwordx4 v94, s[64:65]\n"
"s_mov_b32 m0, s27\n"
"s_add_u32 s27, s27, 1024\n"
"global_load_lds_dwordx4 v95, s[64:65]\n"
"s_mov_b32 m0, s27\n"
"s_add_u32 s27, s27, 1024\n"
"global_load_lds_dwordx4 v96, s[64:65]\n"
"s_mov_b32 m0, s27\n"
"s_nop 0\n"
"global_load_lds_dwordx4 v97, s[64:65]\n"
"s_add_u32 s64, s64, 0x5000\n"
"s_addc_u32 s65, s65, 0\n"
"v_lshrrev_b32 v100, 2, v99\n"
"v_and_b32 v101, 7, v100\n"
"v_lshlrev_b32 v101, 1, v101\n"
"v_mul_u32_u24 v100, 1280, v100\n"
"v_and_b32 v102, 1, v99\n"
"v_lshl_add_u32 v100, v102, 3, v100\n"
"v_bfe_u32 v102, v99, 1, 1\n"
"s_mul_i32 s28, s22, 20\n"
"s_add_u32 s28, s28, 0\n"
"v_add_u32 v103, s28, v102\n"
"v_xor_b32 v103, v103, v101\n"
"v_lshl_add_u32 v84, v103, 4, v100\n"
"s_mul_i32 s28, s22, 20\n"
"s_add_u32 s28, s28, 2\n"
"v_add_u32 v103, s28, v102\n"
"v_xor_b32 v103, v103, v101\n"
"v_lshl_add_u32 v85, v103, 4, v100\n"
"s_mul_i32 s28, s22, 20\n"
"s_add_u32 s28, s28, 4\n"
"v_add_u32 v103, s28, v102\n"
"v_xor_b32 v103, v103, v101\n"
"v_lshl_add_u32 v86, v103, 4, v100\n"
"s_mul_i32 s28, s22, 20\n"
"s_add_u32 s28, s28, 6\n"
"v_add_u32 v103, s28, v102\n"
"v_xor_b32 v103, v103, v101\n"
"v_lshl_add_u32 v87, v103, 4, v100\n"
"s_mul_i32 s28, s22, 20\n"
"s_add_u32 s28, s28, 8\n"
"v_add_u32 v103, s28, v102\n"
"v_xor_b32 v103, v103, v101\n"
"v_lshl_add_u32 v88, v103, 4, v100\n"
"s_mul_i32 s28, s22, 20\n"
"s_add_u32 s28, s28, 10\n"
"v_add_u32 v103, s28, v102\n"
"v_xor_b32 v103, v103, v101\n"
"v_lshl_add_u32 v89, v103, 4, v100\n"
"s_mul_i32 s28, s22, 20\n"
"s_add_u32 s28, s28, 12\n"
"v_add_u32 v103, s28, v102\n"
"v_xor_b32 v103, v103, v101\n"
"v_lshl_add_u32 v90, v103, 4, v100\n"
"s_mul_i32 s28, s22, 20\n"
"s_add_u32 s28, s28, 14\n"
"v_add_u32 v103, s28, v102\n"
"v_xor_b32 v103, v103, v101\n"
"v_lshl_add_u32 v91, v103, 4, v100\n"
"s_mul_i32 s28, s22, 20\n"
"s_add_u32 s28, s28, 16\n"
"v_add_u32 v103, s28, v102\n"
"v_xor_b32 v103, v103, v101\n"
"v_lshl_add_u32 v92, v103, 4, v100\n"
"s_mul_i32 s28, s22, 20\n"
"s_add_u32 s28, s28, 18\n"
"v_add_u32 v103, s28, v102\n"
"v_xor_b32 v103, v103, v101\n"
"v_lshl_add_u32 v98, v103, 4, v100\n"
"ds_read_b64_tr_b16 v[12:13], v84 offset:0\n"
"ds_read_b64_tr_b16 a[240:241], v85 offset:0\n"
"ds_read_b64_tr_b16 v[112:113], v86 offset:0\n"
"ds_read_b64_tr_b16 v[136:137], v87 offset:0\n"
"ds_read_b64_tr_b16 v[36:37], v88 offset:0\n"
"ds_read_b64_tr_b16 v[160:161], v89 offset:0\n"
"ds_read_b64_tr_b16 v[184:185], v90 offset:0\n"
"ds_read_b64_tr_b16 v[208:209], v91 offset:0\n"
"ds_read_b64_tr_b16 v[60:61], v92 offset:0\n"
"ds_read_b64_tr_b16 v[232:233], v98 offset:0\n"
"s_waitcnt vmcnt(15)\n"
"s_waitcnt lgkmcnt(0)\n"
"s_barrier\n"
"s_mul_i32 s27, s22, 5120\n"
"s_add_u32 s27, s27, 0\n"
"s_mov_b32 m0, s27\n"
"s_add_u32 s27, s27, 1024\n"
"global_load_lds_dwordx4 v93, s[64:65]\n"
"s_mov_b32 m0, s27\n"
"s_add_u32 s27, s27, 1024\n"
"global_load_lds_dwordx4 v94, s[64:65]\n"
"s_mov_b32 m0, s27\n"
"s_add_u32 s27, s27, 1024\n"
"global_load_lds_dwordx4 v95, s[64:65]\n"
"s_mov_b32 m0, s27\n"
"s_add_u32 s27, s27, 1024\n"
"global_load_lds_dwordx4 v96, s[64:65]\n"
"s_mov_b32 m0, s27\n"
"s_nop 0\n"
"global_load_lds_dwordx4 v97, s[64:65]\n"
"s_add_u32 s64, s64, 0x5000\n"
"s_addc_u32 s65, s65, 0\n"
"ds_read_b64_tr_b16 v[14:15], v84 offset:24576\n"
"ds_read_b64_tr_b16 a[242:243], v85 offset:24576\n"
"ds_read_b64_tr_b16 v[114:115], v86 offset:24576\n"
"ds_read_b64_tr_b16 v[138:139], v87 offset:24576\n"
"ds_read_b64_tr_b16 v[38:39], v88 offset:24576\n"
"ds_read_b64_tr_b16 v[162:163], v89 offset:24576\n"
"ds_read_b64_tr_b16 v[186:187], v90 offset:24576\n"
"ds_read_b64_tr_b16 v[210:211], v91 offset:24576\n"
"ds_read_b64_tr_b16 v[62:63], v92 offset:24576\n"
"ds_read_b64_tr_b16 v[234:235], v98 offset:24576\n"
"s_waitcnt vmcnt(15)\n"
"s_waitcnt lgkmcnt(0)\n"
"s_barrier\n"
"s_mul_i32 s27, s22, 5120\n"
"s_add_u32 s27, s27, 24576\n"
"s_mov_b32 m0, s27\n"
"s_add_u32 s27, s27, 1024\n"
"global_load_lds_dwordx4 v93, s[64:65]\n"
"s_mov_b32 m0, s27\n"
"s_add_u32 s27, s27, 1024\n"
"global_load_lds_dwordx4 v94, s[64:65]\n"
"s_mov_b32 m0, s27\n"
"s_add_u32 s27, s27, 1024\n"
"global_load_lds_dwordx4 v95, s[64:65]\n"
"s_mov_b32 m0, s27\n"
"s_add_u32 s27, s27, 1024\n"
"global_load_lds_dwordx4 v96, s[64:65]\n"
"s_mov_b32 m0, s27\n"
"s_nop 0\n"
"global_load_lds_dwordx4 v97, s[64:65]\n"
"s_add_u32 s64, s64, 0x5000\n"
"s_addc_u32 s65, s65, 0\n"
"ds_read_b64_tr_b16 v[16:17], v84 offset:49152\n"
"ds_read_b64_tr_b16 a[244:245], v85 offset:49152\n"
"ds_read_b64_tr_b16 v[116:117], v86 offset:49152\n"
"ds_read_b64_tr_b16 v[140:141], v87 offset:49152\n"
"ds_read_b64_tr_b16 v[40:41], v88 offset:49152\n"
"ds_read_b64_tr_b16 v[164:165], v89 offset:49152\n"
"ds_read_b64_tr_b16 v[188:189], v90 offset:49152\n"
"ds_read_b64_tr_b16 v[212:213], v91 offset:49152\n"
"ds_read_b64_tr_b16 v[64:65], v92 offset:49152\n"
"ds_read_b64_tr_b16 v[236:237], v98 offset:49152\n"
"s_waitcnt vmcnt(15)\n"
"s_waitcnt lgkmcnt(0)\n"
"s_barrier\n"
"global_load_dwordx2 v[10:11], v9, s[92:93] sc1\n"
"s_mul_i32 s27, s22, 5120\n"
"s_add_u32 s27, s27, 49152\n"
"s_mov_b32 m0, s27\n"
"s_add_u32 s27, s27, 1024\n"
"global_load_lds_dwordx4 v93, s[64:65]\n"
"s_mov_b32 m0, s27\n"
"s_add_u32 s27, s27, 1024\n"
"global_load_lds_dwordx4 v94, s[64:65]\n"
"s_mov_b32 m0, s27\n"
"s_add_u32 s27, s27, 1024\n"
"global_load_lds_dwordx4 v95, s[64:65]\n"
"s_mov_b32 m0, s27\n"
"s_add_u32 s27, s27, 1024\n"
"global_load_lds_dwordx4 v96, s[64:65]\n"
"s_mov_b32 m0, s27\n"
"s_nop 0\n"
"global_load_lds_dwordx4 v97, s[64:65]\n"
"s_add_u32 s64, s64, 0x5000\n"
"s_addc_u32 s65, s65, 0\n"
"v_add_u32 v84, 0x1c000, v84\n"
"v_add_u32 v85, 0x1c000, v85\n"
"v_add_u32 v86, 0x1c000, v86\n"
"v_add_u32 v87, 0x1c000, v87\n"
"v_add_u32 v88, 0x1c000, v88\n"
"v_add_u32 v89, 0x1c000, v89\n"
"v_add_u32 v90, 0x1c000, v90\n"
"v_add_u32 v91, 0x1c000, v91\n"
"v_add_u32 v92, 0x1c000, v92\n"
"v_add_u32 v98, 0x1c000, v98\n"
"ds_read_b64_tr_b16 v[18:19], v84 offset:0\n"
"ds_read_b64_tr_b16 a[246:247], v85 offset:0\n"
"ds_read_b64_tr_b16 v[118:119], v86 offset:0\n"
"ds_read_b64_tr_b16 v[142:143], v87 offset:0\n"
"ds_read_b64_tr_b16 v[42:43], v88 offset:0\n"
"ds_read_b64_tr_b16 v[166:167], v89 offset:0\n"
"ds_read_b64_tr_b16 v[190:191], v90 offset:0\n"
"ds_read_b64_tr_b16 v[214:215], v91 offset:0\n"
"ds_read_b64_tr_b16 v[66:67], v92 offset:0\n"
"ds_read_b64_tr_b16 v[238:239], v98 offset:0\n"
"s_waitcnt vmcnt(15)\n"
"s_waitcnt lgkmcnt(0)\n"
"s_barrier\n"
"s_mul_i32 s27, s22, 5120\n"
"s_add_u32 s27, s27, 114688\n"
"s_mov_b32 m0, s27\n"
"s_add_u32 s27, s27, 1024\n"
"global_load_lds_dwordx4 v93, s[64:65]\n"
"s_mov_b32 m0, s27\n"
"s_add_u32 s27, s27, 1024\n"
"global_load_lds_dwordx4 v94, s[64:65]\n"
"s_mov_b32 m0, s27\n"
"s_add_u32 s27, s27, 1024\n"
"global_load_lds_dwordx4 v95, s[64:65]\n"
"s_mov_b32 m0, s27\n"
"s_add_u32 s27, s27, 1024\n"
"global_load_lds_dwordx4 v96, s[64:65]\n"
"s_mov_b32 m0, s27\n"
"s_nop 0\n"
"global_load_lds_dwordx4 v97, s[64:65]\n"
"s_add_u32 s64, s64, 0x5000\n"
"s_addc_u32 s65, s65, 0\n"
"ds_read_b64_tr_b16 v[20:21], v84 offset:24576\n"
"ds_read_b64_tr_b16 a[248:249], v85 offset:24576\n"
"ds_read_b64_tr_b16 v[120:121], v86 offset:24576\n"
"ds_read_b64_tr_b16 v[144:145], v87 offset:24576\n"
"ds_read_b64_tr_b16 v[44:45], v88 offset:24576\n"
"ds_read_b64_tr_b16 v[168:169], v89 offset:24576\n"
"ds_read_b64_tr_b16 v[192:193], v90 offset:24576\n"
"ds_read_b64_tr_b16 v[216:217], v91 offset:24576\n"
"ds_read_b64_tr_b16 v[68:69], v92 offset:24576\n"
"ds_read_b64_tr_b16 v[240:241], v98 offset:24576\n"
"s_waitcnt vmcnt(15)\n"
"s_waitcnt lgkmcnt(0)\n"
"s_barrier\n"
"s_mul_i32 s27, s22, 5120\n"
"s_add_u32 s27, s27, 139264\n"
"s_mov_b32 m0, s27\n"
"s_add_u32 s27, s27, 1024\n"
"global_load_lds_dwordx4 v93, s[64:65]\n"
"s_mov_b32 m0, s27\n"
"s_add_u32 s27, s27, 1024\n"
"global_load_lds_dwordx4 v94, s[64:65]\n"
"s_mov_b32 m0, s27\n"
"s_add_u32 s27, s27, 1024\n"
"global_load_lds_dwordx4 v95, s[64:65]\n"
"s_mov_b32 m0, s27\n"
"s_add_u32 s27, s27, 1024\n"
"global_load_lds_dwordx4 v96, s[64:65]\n"
"s_mov_b32 m0, s27\n"
"s_nop 0\n"
"global_load_lds_dwordx4 v97, s[64:65]\n"
"s_add_u32 s64, s64, 0x5000\n"
"s_addc_u32 s65, s65, 0\n"
"v_subrev_u32 v84, 0x1c000, v84\n"
"v_subrev_u32 v85, 0x1c000, v85\n"
"v_subrev_u32 v86, 0x1c000, v86\n"
"v_subrev_u32 v87, 0x1c000, v87\n"
"v_subrev_u32 v88, 0x1c000, v88\n"
"v_subrev_u32 v89, 0x1c000, v89\n"
"v_subrev_u32 v90, 0x1c000, v90\n"
"v_subrev_u32 v91, 0x1c000, v91\n"
"v_subrev_u32 v92, 0x1c000, v92\n"
"v_subrev_u32 v98, 0x1c000, v98\n"
"ds_read_b64_tr_b16 v[22:23], v84 offset:0\n"
"ds_read_b64_tr_b16 a[250:251], v85 offset:0\n"
"ds_read_b64_tr_b16 v[122:123], v86 offset:0\n"
"ds_read_b64_tr_b16 v[146:147], v87 offset:0\n"
"ds_read_b64_tr_b16 v[46:47], v88 offset:0\n"
"ds_read_b64_tr_b16 v[170:171], v89 offset:0\n"
"ds_read_b64_tr_b16 v[194:195], v90 offset:0\n"
"ds_read_b64_tr_b16 v[218:219], v91 offset:0\n"
"ds_read_b64_tr_b16 v[70:71], v92 offset:0\n"
"ds_read_b64_tr_b16 v[242:243], v98 offset:0\n"
"s_waitcnt vmcnt(15)\n"
"s_waitcnt lgkmcnt(0)\n"
"s_barrier\n"
"s_mul_i32 s27, s22, 5120\n"
"s_add_u32 s27, s27, 0\n"
"s_mov_b32 m0, s27\n"
"s_add_u32 s27, s27, 1024\n"
"global_load_lds_dwordx4 v93, s[64:65]\n"
"s_mov_b32 m0, s27\n"
"s_add_u32 s27, s27, 1024\n"
"global_load_lds_dwordx4 v94, s[64:65]\n"
"s_mov_b32 m0, s27\n"
"s_add_u32 s27, s27, 1024\n"
"global_load_lds_dwordx4 v95, s[64:65]\n"
"s_mov_b32 m0, s27\n"
"s_add_u32 s27, s27, 1024\n"
"global_load_lds_dwordx4 v96, s[64:65]\n"
"s_mov_b32 m0, s27\n"
"s_nop 0\n"
"global_load_lds_dwordx4 v97, s[64:65]\n"
"s_add_u32 s64, s64, 0x5000\n"
"s_addc_u32 s65, s65, 0\n"
"ds_read_b64_tr_b16 v[24:25], v84 offset:24576\n"
"ds_read_b64_tr_b16 a[252:253], v85 offset:24576\n"
"ds_read_b64_tr_b16 v[124:125], v86 offset:24576\n"
"ds_read_b64_tr_b16 v[148:149], v87 offset:24576\n"
"ds_read_b64_tr_b16 v[48:49], v88 offset:24576\n"
"ds_read_b64_tr_b16 v[172:173], v89 offset:24576\n"
"ds_read_b64_tr_b16 v[196:197], v90 offset:24576\n"
"ds_read_b64_tr_b16 v[220:221], v91 offset:24576\n"
"ds_read_b64_tr_b16 v[72:73], v92 offset:24576\n"
"ds_read_b64_tr_b16 v[244:245], v98 offset:24576\n"
"s_waitcnt vmcnt(15)\n"
"s_waitcnt lgkmcnt(0)\n"
"s_barrier\n"
"s_mul_i32 s27, s22, 5120\n"
"s_add_u32 s27, s27, 24576\n"
"s_mov_b32 m0, s27\n"
"s_add_u32 s27, s27, 1024\n"
"global_load_lds_dwordx4 v93, s[64:65]\n"
"s_mov_b32 m0, s27\n"
"s_add_u32 s27, s27, 1024\n"
"global_load_lds_dwordx4 v94, s[64:65]\n"
"s_mov_b32 m0, s27\n"
"s_add_u32 s27, s27, 1024\n"
"global_load_lds_dwordx4 v95, s[64:65]\n"
"s_mov_b32 m0, s27\n"
"s_add_u32 s27, s27, 1024\n"
"global_load_lds_dwordx4 v96, s[64:65]\n"
"s_mov_b32 m0, s27\n"
"s_nop 0\n"
"global_load_lds_dwordx4 v97, s[64:65]\n"
"s_add_u32 s64, s64, 0x5000\n"
"s_addc_u32 s65, s65, 0\n"
".Lv_hs_1:\n"
"v_cmp_ne_u32 vcc, v8, v10\n"
"s_cbranch_vccz .Lv_hso_2\n"
"global_load_dwordx2 v[10:11], v9, s[92:93] sc1\n"
"s_waitcnt vmcnt(0)\n"
"s_add_u32 s26, s26, 1\n"
"s_cmp_lt_u32 s26, 0x80000\n"
"s_cbranch_scc1 .Lv_hs_1\n"
"s_branch .Lv_abort\n"
".Lv_hso_2:\n"
"v_readfirstlane_b32 s68, v11\n"
"v_xor_b32 v8, 0x80000000, v8\n"
"s_mov_b64 exec, 1\n"
"global_store_dword v9, v8, s[90:91] sc1\n"
"s_mov_b64 exec, -1\n"
"s_mov_b32 s70, 0\n"
"s_lshr_b32 s27, s68, 0\n"
"s_xor_b32 s27, s27, s24\n"
"s_and_b32 s27, s27, 1\n"
"s_lshl_b32 s27, s27, 13\n"
"s_add_u32 s27, s27, s70\n"
"s_add_u32 s56, s20, s27\n"
"s_addc_u32 s57, s21, 0\n"
"s_add_u32 s27, s70, 0x618000\n"
"s_add_u32 s30, s20, s27\n"
"s_addc_u32 s31, s21, 0\n"
"s_cmp_eq_u32 s24, 0\n"
"s_cselect_b64 s[30:31], s[30:31], s[56:57]\n"
"s_lshr_b32 s27, s68, 0\n"
"s_xor_b32 s27, s27, s24\n"
"s_and_b32 s27, s27, 1\n"
"s_lshl_b32 s27, s27, 13\n"
"s_add_u32 s27, s27, 0x180000\n"
"s_mul_i32 s28, s23, 8192\n"
"s_lshr_b32 s28, s28, 16\n"
"s_lshl_b32 s28, s28, 8\n"
"s_add_u32 s27, s27, s28\n"
"s_add_u32 s98, s20, s27\n"
"s_addc_u32 s99, s21, 0\n"
"s_mul_i32 s27, s24, 40960\n"
"s_mul_i32 s28, s22, 10240\n"
"s_add_u32 s27, s27, s28\n"
"s_add_u32 s27, s27, 0x800000\n"
"s_add_u32 s96, s20, s27\n"
"s_addc_u32 s97, s21, 0\n"
"s_mul_i32 s27, s22, 10240\n"
"s_add_u32 s27, s27, 73728\n"
"s_mov_b32 m0, s27\n"
"s_add_u32 s28, s96, 0x0\n"
"s_addc_u32 s29, s97, 0\n"
"global_load_lds_dwordx4 v0, s[28:29] offset:0 sc1\n"
"global_load_lds_dwordx4 v0, s[28:29] offset:1024 sc1\n"
"global_load_lds_dwordx4 v0, s[28:29] offset:2048 sc1\n"
"global_load_lds_dwordx4 v0, s[28:29] offset:3072 sc1\n"
"s_mul_i32 s27, s22, 10240\n"
"s_add_u32 s27, s27, 77824\n"
"s_mov_b32 m0, s27\n"
"s_add_u32 s28, s96, 0x1000\n"
"s_addc_u32 s29, s97, 0\n"
"global_load_lds_dwordx4 v0, s[28:29] offset:0 sc1\n"
"global_load_lds_dwordx4 v0, s[28:29] offset:1024 sc1\n"
"global_load_lds_dwordx4 v0, s[28:29] offset:2048 sc1\n"
"global_load_lds_dwordx4 v0, s[28:29] offset:3072 sc1\n"
"s_mul_i32 s27, s22, 10240\n"
"s_add_u32 s27, s27, 81920\n"
"s_mov_b32 m0, s27\n"
"s_add_u32 s28, s96, 0x2000\n"
"s_addc_u32 s29, s97, 0\n"
"global_load_lds_dwordx4 v0, s[28:29] offset:0 sc1\n"
"global_load_lds_dwordx4 v0, s[28:29] offset:1024 sc1\n"
"s_add_u32 s96, s96, 0x14000\n"
"s_addc_u32 s97, s97, 0\n"
"s_mul_i32 s27, s24, 2621440\n"
"s_mul_i32 s28, s22, 327680\n"
"s_add_u32 s27, s27, s28\n"
"s_lshl_b32 s28, s23, 10\n"
"s_add_u32 s27, s27, s28\n"
"s_add_u32 s27, s27, 0xc00000\n"
"s_add_u32 s34, s20, s27\n"
"s_addc_u32 s35, s21, 0\n"
"s_add_u32 s60, s34, 0x140000\n"
"s_addc_u32 s61, s35, 0\n"
"s_lshr_b32 s27, s68, 2\n"
"s_xor_b32 s27, s27, s24\n"
"s_and_b32 s27, s27, 1\n"
"s_lshl_b32 s27, s27, 13\n"
"s_mul_i32 s28, s22, 163840\n"
"s_add_u32 s27, s27, s28\n"
"s_lshl_b32 s28, s23, 8\n"
"s_add_u32 s27, s27, s28\n"
"s_add_u32 s27, s27, 0x400000\n"
"s_add_u32 s36, s20, s27\n"
"s_addc_u32 s37, s21, 0\n"
"s_add_u32 s62, s36, 0xa0000\n"
"s_addc_u32 s63, s37, 0\n"
"s_lshl_b32 s27, s24, 2\n"
"s_add_u32 s27, s27, s22\n"
"s_lshl_b32 s27, s27, 5\n"
"s_add_u32 s27, s27, s23\n"
"s_lshl_b32 s27, s27, 7\n"
"s_add_u32 s27, s27, 0x600000\n"
"s_add_u32 s38, s20, s27\n"
"s_addc_u32 s39, s21, 0\n"
"ds_read_b64_tr_b16 v[26:27], v84 offset:49152\n"
"ds_read_b64_tr_b16 a[254:255], v85 offset:49152\n"
"ds_read_b64_tr_b16 v[126:127], v86 offset:49152\n"
"ds_read_b64_tr_b16 v[150:151], v87 offset:49152\n"
"ds_read_b64_tr_b16 v[50:51], v88 offset:49152\n"
"ds_read_b64_tr_b16 v[174:175], v89 offset:49152\n"
"ds_read_b64_tr_b16 v[198:199], v90 offset:49152\n"
"ds_read_b64_tr_b16 v[222:223], v91 offset:49152\n"
"ds_read_b64_tr_b16 v[74:75], v92 offset:49152\n"
"ds_read_b64_tr_b16 v[246:247], v98 offset:49152\n"
"s_waitcnt vmcnt(26)\n"
"s_waitcnt lgkmcnt(0)\n"
"s_barrier\n"
"v_add_u32 v84, 0x1c000, v84\n"
"v_add_u32 v85, 0x1c000, v85\n"
"v_add_u32 v86, 0x1c000, v86\n"
"v_add_u32 v87, 0x1c000, v87\n"
"v_add_u32 v88, 0x1c000, v88\n"
"v_add_u32 v89, 0x1c000, v89\n"
"v_add_u32 v90, 0x1c000, v90\n"
"v_add_u32 v91, 0x1c000, v91\n"
"v_add_u32 v92, 0x1c000, v92\n"
"v_add_u32 v98, 0x1c000, v98\n"
"ds_read_b64_tr_b16 v[28:29], v84 offset:0\n"
"ds_read_b64_tr_b16 v[104:105], v85 offset:0\n"
"ds_read_b64_tr_b16 v[128:129], v86 offset:0\n"
"ds_read_b64_tr_b16 v[152:153], v87 offset:0\n"
"ds_read_b64_tr_b16 v[52:53], v88 offset:0\n"
"ds_read_b64_tr_b16 v[176:177], v89 offset:0\n"
"ds_read_b64_tr_b16 v[200:201], v90 offset:0\n"
"ds_read_b64_tr_b16 v[224:225], v91 offset:0\n"
"ds_read_b64_tr_b16 v[76:77], v92 offset:0\n"
"ds_read_b64_tr_b16 v[248:249], v98 offset:0\n"
"s_waitcnt vmcnt(21)\n"
"s_waitcnt lgkmcnt(0)\n"
"s_barrier\n"
"ds_read_b64_tr_b16 v[30:31], v84 offset:24576\n"
"ds_read_b64_tr_b16 v[106:107], v85 offset:24576\n"
"ds_read_b64_tr_b16 v[130:131], v86 offset:24576\n"
"ds_read_b64_tr_b16 v[154:155], v87 offset:24576\n"
"ds_read_b64_tr_b16 v[54:55], v88 offset:24576\n"
"ds_read_b64_tr_b16 v[178:179], v89 offset:24576\n"
"ds_read_b64_tr_b16 v[202:203], v90 offset:24576\n"
"ds_read_b64_tr_b16 v[226:227], v91 offset:24576\n"
"ds_read_b64_tr_b16 v[78:79], v92 offset:24576\n"
"ds_read_b64_tr_b16 v[250:251], v98 offset:24576\n"
"s_waitcnt vmcnt(16)\n"
"s_waitcnt lgkmcnt(0)\n"
"s_barrier\n"
"v_subrev_u32 v84, 0x1c000, v84\n"
"v_subrev_u32 v85, 0x1c000, v85\n"
"v_subrev_u32 v86, 0x1c000, v86\n"
"v_subrev_u32 v87, 0x1c000, v87\n"
"v_subrev_u32 v88, 0x1c000, v88\n"
"v_subrev_u32 v89, 0x1c000, v89\n"
"v_subrev_u32 v90, 0x1c000, v90\n"
"v_subrev_u32 v91, 0x1c000, v91\n"
"v_subrev_u32 v92, 0x1c000, v92\n"
"v_subrev_u32 v98, 0x1c000, v98\n"
"ds_read_b64_tr_b16 v[32:33], v84 offset:0\n"
"ds_read_b64_tr_b16 v[108:109], v85 offset:0\n"
"ds_read_b64_tr_b16 v[132:133], v86 offset:0\n"
"ds_read_b64_tr_b16 v[156:157], v87 offset:0\n"
"ds_read_b64_tr_b16 v[56:57], v88 offset:0\n"
"ds_read_b64_tr_b16 v[180:181], v89 offset:0\n"
"ds_read_b64_tr_b16 v[204:205], v90 offset:0\n"
"ds_read_b64_tr_b16 v[228:229], v91 offset:0\n"
"ds_read_b64_tr_b16 v[80:81], v92 offset:0\n"
"ds_read_b64_tr_b16 v[252:253], v98 offset:0\n"
"s_waitcnt vmcnt(11)\n"
"s_waitcnt lgkmcnt(0)\n"
"s_barrier\n"
"ds_read_b64_tr_b16 v[34:35], v84 offset:24576\n"
"ds_read_b64_tr_b16 v[110:111], v85 offset:24576\n"
"ds_read_b64_tr_b16 v[134:135], v86 offset:24576\n"
"ds_read_b64_tr_b16 v[158:159], v87 offset:24576\n"
"ds_read_b64_tr_b16 v[58:59], v88 offset:24576\n"
"ds_read_b64_tr_b16 v[182:183], v89 offset:24576\n"
"ds_read_b64_tr_b16 v[206:207], v90 offset:24576\n"
"ds_read_b64_tr_b16 v[230:231], v91 offset:24576\n"
"ds_read_b64_tr_b16 v[82:83], v92 offset:24576\n"
"ds_read_b64_tr_b16 v[254:255], v98 offset:24576\n"
"s_waitcnt vmcnt(0)\n"
"s_waitcnt lgkmcnt(0)\n"
"s_barrier\n"
"ds_write_b128 v4, v[12:15] offset:0\n"
"ds_write_b128 v4, v[16:19] offset:1024\n"
"ds_write_b128 v4, v[20:23] offset:2048\n"
"ds_write_b128 v4, v[24:27] offset:3072\n"
"ds_write_b128 v4, v[28:31] offset:4096\n"
"ds_write_b128 v4, v[32:35] offset:5120\n"
"ds_write_b128 v4, v[36:39] offset:6144\n"
"ds_write_b128 v4, v[40:43] offset:7168\n"
"ds_write_b128 v4, v[44:47] offset:8192\n"
"ds_write_b128 v4, v[48:51] offset:9216\n"
"ds_write_b128 v4, v[52:55] offset:10240\n"
"ds_write_b128 v4, v[56:59] offset:11264\n"
"ds_write_b128 v4, v[60:63] offset:12288\n"
"ds_write_b128 v4, v[64:67] offset:13312\n"
"ds_write_b128 v4, v[68:71] offset:14336\n"
"ds_write_b128 v4, v[72:75] offset:15360\n"
"ds_write_b128 v4, v[76:79] offset:16384\n"
"ds_write_b128 v4, v[80:83] offset:17408\n"
"s_waitcnt lgkmcnt(0)\n"
"s_barrier\n"
".Lv_top_3:\n"
"s_nop 7\n"
"ds_read_b128 v[34:37], v5 offset:0\n"
"ds_read_b128 v[38:41], v5 offset:1024\n"
"ds_read_b128 v[42:45], v5 offset:2048\n"
"ds_read_b128 v[46:49], v5 offset:3072\n"
"s_mul_i32 s27, s22, 3\n"
"s_add_u32 s27, s27, 0\n"
"s_and_b32 s28, s27, 1\n"
"s_lshl_b32 s28, s28, 3\n"
"s_lshr_b32 s27, s27, 1\n"
"s_lshl_b32 s27, s27, 10\n"
"s_add_u32 s27, s27, s28\n"
"v_add_u32 v60, s27, v5\n"
"s_mul_i32 s27, s22, 3\n"
"s_add_u32 s27, s27, 1\n"
"s_and_b32 s28, s27, 1\n"
"s_lshl_b32 s28, s28, 3\n"
"s_lshr_b32 s27, s27, 1\n"
"s_lshl_b32 s27, s27, 10\n"
"s_add_u32 s27, s27, s28\n"
"v_add_u32 v61, s27, v5\n"
"s_mul_i32 s27, s22, 3\n"
"s_add_u32 s27, s27, 2\n"
"s_and_b32 s28, s27, 1\n"
"s_lshl_b32 s28, s28, 3\n"
"s_lshr_b32 s27, s27, 1\n"
"s_lshl_b32 s27, s27, 10\n"
"s_add_u32 s27, s27, s28\n"
"v_add_u32 v62, s27, v5\n"
"s_waitcnt lgkmcnt(3)\n"
"v_mfma_f32_16x16x32_bf16 v[10:13], a[0:3], v[34:37], 0\n"
"ds_read_b128 v[50:53], v5 offset:4096\n"
"v_mfma_f32_16x16x32_bf16 v[14:17], a[8:11], v[34:37], 0\n"
"ds_read_b128 v[54:57], v5 offset:5120\n"
"v_mfma_f32_16x16x32_bf16 v[18:21], a[16:19], v[34:37], 0\n"
"s_waitcnt lgkmcnt(4)\n"
"v_mfma_f32_16x16x32_bf16 v[22:25], a[0:3], v[38:41], 0\n"
"v_mfma_f32_16x16x32_bf16 v[26:29], a[8:11], v[38:41], 0\n"
"v_mfma_f32_16x16x32_bf16 v[30:33], a[16:19], v[38:41], 0\n"
"s_waitcnt lgkmcnt(3)\n"
"v_mfma_f32_16x16x32_bf16 v[10:13], a[4:7], v[42:45], v[10:13]\n"
"ds_read_b128 v[34:37], v5 offset:6144\n"
"v_mfma_f32_16x16x32_bf16 v[14:17], a[12:15], v[42:45], v[14:17]\n"
"ds_read_b128 v[38:41], v5 offset:7168\n"
"v_mfma_f32_16x16x32_bf16 v[18:21], a[20:23], v[42:45], v[18:21]\n"
"s_waitcnt lgkmcnt(4)\n"
"v_mfma_f32_16x16x32_bf16 v[22:25], a[4:7], v[46:49], v[22:25]\n"
"v_mfma_f32_16x16x32_bf16 v[26:29], a[12:15], v[46:49], v[26:29]\n"
"v_mfma_f32_16x16x32_bf16 v[30:33], a[20:23], v[46:49], v[30:33]\n"
"s_waitcnt lgkmcnt(3)\n"
"v_mfma_f32_16x16x32_bf16 v[10:13], a[24:27], v[50:53], v[10:13]\n"
"ds_read_b128 v[42:45], v5 offset:8192\n"
"v_mfma_f32_16x16x32_bf16 v[14:17], a[32:35], v[50:53], v[14:17]\n"
"ds_read_b128 v[46:49], v5 offset:9216\n"
"v_mfma_f32_16x16x32_bf16 v[18:21], a[40:43], v[50:53], v[18:21]\n"
"s_waitcnt lgkmcnt(4)\n"
"v_mfma_f32_16x16x32_bf16 v[22:25], a[24:27], v[54:57], v[22:25]\n"
"v_mfma_f32_16x16x32_bf16 v[26:29], a[32:35], v[54:57], v[26:29]\n"
"v_mfma_f32_16x16x32_bf16 v[30:33], a[40:43], v[54:57], v[30:33]\n"
"s_waitcnt lgkmcnt(3)\n"
"v_mfma_f32_16x16x32_bf16 v[10:13], a[28:31], v[34:37], v[10:13]\n"
"ds_read_b128 v[50:53], v5 offset:10240\n"
"v_mfma_f32_16x16x32_bf16 v[14:17], a[36:39], v[34:37], v[14:17]\n"
"ds_read_b128 v[54:57], v5 offset:11264\n"
"v_mfma_f32_16x16x32_bf16 v[18:21], a[44:47], v[34:37], v[18:21]\n"
"s_waitcnt lgkmcnt(4)\n"
"v_mfma_f32_16x16x32_bf16 v[22:25], a[28:31], v[38:41], v[22:25]\n"
"v_mfma_f32_16x16x32_bf16 v[26:29], a[36:39], v[38:41], v[26:29]\n"
"v_mfma_f32_16x16x32_bf16 v[30:33], a[44:47], v[38:41], v[30:33]\n"
"s_waitcnt lgkmcnt(3)\n"
"v_mfma_f32_16x16x32_bf16 v[10:13], a[48:51], v[42:45], v[10:13]\n"
"ds_read_b128 v[34:37], v5 offset:12288\n"
"v_mfma_f32_16x16x32_bf16 v[14:17], a[56:59], v[42:45], v[14:17]\n"
"ds_read_b128 v[38:41], v5 offset:13312\n"
"v_mfma_f32_16x16x32_bf16 v[18:21], a[64:67], v[42:45], v[18:21]\n"
"s_waitcnt lgkmcnt(4)\n"
"v_mfma_f32_16x16x32_bf16 v[22:25], a[48:51], v[46:49], v[22:25]\n"
"v_mfma_f32_16x16x32_bf16 v[26:29], a[56:59], v[46:49], v[26:29]\n"
"v_mfma_f32_16x16x32_bf16 v[30:33], a[64:67], v[46:49], v[30:33]\n"
"s_waitcnt lgkmcnt(3)\n"
"v_mfma_f32_16x16x32_bf16 v[10:13], a[52:55], v[50:53], v[10:13]\n"
"ds_read_b128 v[42:45], v5 offset:14336\n"
"v_mfma_f32_16x16x32_bf16 v[14:17], a[60:63], v[50:53], v[14:17]\n"
"ds_read_b128 v[46:49], v5 offset:15360\n"
"v_mfma_f32_16x16x32_bf16 v[18:21], a[68:71], v[50:53], v[18:21]\n"
"s_waitcnt lgkmcnt(4)\n"
"v_mfma_f32_16x16x32_bf16 v[22:25], a[52:55], v[54:57], v[22:25]\n"
"v_mfma_f32_16x16x32_bf16 v[26:29], a[60:63], v[54:57], v[26:29]\n"
"v_mfma_f32_16x16x32_bf16 v[30:33], a[68:71], v[54:57], v[30:33]\n"
"s_waitcnt lgkmcnt(3)\n"
"v_mfma_f32_16x16x32_bf16 v[10:13], a[72:75], v[34:37], v[10:13]\n"
"ds_read_b128 v[50:53], v5 offset:16384\n"
"v_mfma_f32_16x16x32_bf16 v[14:17], a[80:83], v[34:37], v[14:17]\n"
"ds_read_b128 v[54:57], v5 offset:17408\n"
"v_mfma_f32_16x16x32_bf16 v[18:21], a[88:91], v[34:37], v[18:21]\n"
"s_waitcnt lgkmcnt(4)\n"
"v_mfma_f32_16x16x32_bf16 v[22:25], a[72:75], v[38:41], v[22:25]\n"
"v_mfma_f32_16x16x32_bf16 v[26:29], a[80:83], v[38:41], v[26:29]\n"
"v_mfma_f32_16x16x32_bf16 v[30:33], a[88:91], v[38:41], v[30:33]\n"
"s_waitcnt lgkmcnt(3)\n"
"v_mfma_f32_16x16x32_bf16 v[10:13], a[76:79], v[42:45], v[10:13]\n"
"ds_read_b128 v[34:37], v5 offset:18432\n"
"v_mfma_f32_16x16x32_bf16 v[14:17], a[84:87], v[42:45], v[14:17]\n"
"ds_read_b128 v[38:41], v5 offset:19456\n"
"v_mfma_f32_16x16x32_bf16 v[18:21], a[92:95], v[42:45], v[18:21]\n"
"s_waitcnt lgkmcnt(4)\n"
"v_mfma_f32_16x16x32_bf16 v[22:25], a[76:79], v[46:49], v[22:25]\n"
"v_mfma_f32_16x16x32_bf16 v[26:29], a[84:87], v[46:49], v[26:29]\n"
"v_mfma_f32_16x16x32_bf16 v[30:33], a[92:95], v[46:49], v[30:33]\n"
"s_waitcnt lgkmcnt(3)\n"
"v_mfma_f32_16x16x32_bf16 v[10:13], a[96:99], v[50:53], v[10:13]\n"
"ds_read_b128 v[42:45], v5 offset:20480\n"
"v_mfma_f32_16x16x32_bf16 v[14:17], a[104:107], v[50:53], v[14:17]\n"
"ds_read_b128 v[46:49], v5 offset:21504\n"
"v_mfma_f32_16x16x32_bf16 v[18:21], a[112:115], v[50:53], v[18:21]\n"
"s_waitcnt lgkmcnt(4)\n"
"v_mfma_f32_16x16x32_bf16 v[22:25], a[96:99], v[54:57], v[22:25]\n"
"v_mfma_f32_16x16x32_bf16 v[26:29], a[104:107], v[54:57], v[26:29]\n"
"v_mfma_f32_16x16x32_bf16 v[30:33], a[112:115], v[54:57], v[30:33]\n"
"s_waitcnt lgkmcnt(3)\n"
"v_mfma_f32_16x16x32_bf16 v[10:13], a[100:103], v[34:37], v[10:13]\n"
"ds_read_b128 v[50:53], v5 offset:22528\n"
"v_mfma_f32_16x16x32_bf16 v[14:17], a[108:111], v[34:37], v[14:17]\n"
"ds_read_b128 v[54:57], v5 offset:23552\n"
"v_mfma_f32_16x16x32_bf16 v[18:21], a[116:119], v[34:37], v[18:21]\n"
"s_waitcnt lgkmcnt(4)\n"
"v_mfma_f32_16x16x32_bf16 v[22:25], a[100:103], v[38:41], v[22:25]\n"
"v_mfma_f32_16x16x32_bf16 v[26:29], a[108:111], v[38:41], v[26:29]\n"
"v_mfma_f32_16x16x32_bf16 v[30:33], a[116:119], v[38:41], v[30:33]\n"
"s_waitcnt lgkmcnt(3)\n"
"v_mfma_f32_16x16x32_bf16 v[10:13], a[120:123], v[42:45], v[10:13]\n"
"ds_read_b128 v[34:37], v5 offset:24576\n"
"v_mfma_f32_16x16x32_bf16 v[14:17], a[128:131], v[42:45], v[14:17]\n"
"ds_read_b128 v[38:41], v5 offset:25600\n"
"v_mfma_f32_16x16x32_bf16 v[18:21], a[136:139], v[42:45], v[18:21]\n"
"s_waitcnt lgkmcnt(4)\n"
"v_mfma_f32_16x16x32_bf16 v[22:25], a[120:123], v[46:49], v[22:25]\n"
"v_mfma_f32_16x16x32_bf16 v[26:29], a[128:131], v[46:49], v[26:29]\n"
"v_mfma_f32_16x16x32_bf16 v[30:33], a[136:139], v[46:49], v[30:33]\n"
"s_waitcnt lgkmcnt(3)\n"
"v_mfma_f32_16x16x32_bf16 v[10:13], a[124:127], v[50:53], v[10:13]\n"
"ds_read_b128 v[42:45], v5 offset:26624\n"
"v_mfma_f32_16x16x32_bf16 v[14:17], a[132:135], v[50:53], v[14:17]\n"
"ds_read_b128 v[46:49], v5 offset:27648\n"
"v_mfma_f32_16x16x32_bf16 v[18:21], a[140:143], v[50:53], v[18:21]\n"
"s_waitcnt lgkmcnt(4)\n"
"v_mfma_f32_16x16x32_bf16 v[22:25], a[124:127], v[54:57], v[22:25]\n"
"v_mfma_f32_16x16x32_bf16 v[26:29], a[132:135], v[54:57], v[26:29]\n"
"v_mfma_f32_16x16x32_bf16 v[30:33], a[140:143], v[54:57], v[30:33]\n"
"s_waitcnt lgkmcnt(3)\n"
"v_mfma_f32_16x16x32_bf16 v[10:13], a[144:147], v[34:37], v[10:13]\n"
"ds_read_b128 v[50:53], v5 offset:28672\n"
"v_mfma_f32_16x16x32_bf16 v[14:17], a[152:155], v[34:37], v[14:17]\n"
"ds_read_b128 v[54:57], v5 offset:29696\n"
"v_mfma_f32_16x16x32_bf16 v[18:21], a[160:163], v[34:37], v[18:21]\n"
"s_waitcnt lgkmcnt(4)\n"
"v_mfma_f32_16x16x32_bf16 v[22:25], a[144:147], v[38:41], v[22:25]\n"
"v_mfma_f32_16x16x32_bf16 v[26:29], a[152:155], v[38:41], v[26:29]\n"
"v_mfma_f32_16x16x32_bf16 v[30:33], a[160:163], v[38:41], v[30:33]\n"
"s_waitcnt lgkmcnt(3)\n"
"v_mfma_f32_16x16x32_bf16 v[10:13], a[148:151], v[42:45], v[10:13]\n"
"ds_read_b128 v[34:37], v5 offset:30720\n"
"v_mfma_f32_16x16x32_bf16 v[14:17], a[156:159], v[42:45], v[14:17]\n"
"ds_read_b128 v[38:41], v5 offset:31744\n"
"v_mfma_f32_16x16x32_bf16 v[18:21], a[164:167], v[42:45], v[18:21]\n"
"s_waitcnt lgkmcnt(4)\n"
"v_mfma_f32_16x16x32_bf16 v[22:25], a[148:151], v[46:49], v[22:25]\n"
"v_mfma_f32_16x16x32_bf16 v[26:29], a[156:159], v[46:49], v[26:29]\n"
"v_mfma_f32_16x16x32_bf16 v[30:33], a[164:167], v[46:49], v[30:33]\n"
"s_waitcnt lgkmcnt(3)\n"
"v_mfma_f32_16x16x32_bf16 v[10:13], a[168:171], v[50:53], v[10:13]\n"
"ds_read_b128 v[42:45], v5 offset:32768\n"
"v_mfma_f32_16x16x32_bf16 v[14:17], a[176:179], v[50:53], v[14:17]\n"
"ds_read_b128 v[46:49], v5 offset:33792\n"
"v_mfma_f32_16x16x32_bf16 v[18:21], a[184:187], v[50:53], v[18:21]\n"
"s_waitcnt lgkmcnt(4)\n"
"v_mfma_f32_16x16x32_bf16 v[22:25], a[168:171], v[54:57], v[22:25]\n"
"v_mfma_f32_16x16x32_bf16 v[26:29], a[176:179], v[54:57], v[26:29]\n"
"v_mfma_f32_16x16x32_bf16 v[30:33], a[184:187], v[54:57], v[30:33]\n"
"s_waitcnt lgkmcnt(3)\n"
"v_mfma_f32_16x16x32_bf16 v[10:13], a[172:175], v[34:37], v[10:13]\n"
"ds_read_b128 v[50:53], v5 offset:34816\n"
"v_mfma_f32_16x16x32_bf16 v[14:17], a[180:183], v[34:37], v[14:17]\n"
"ds_read_b128 v[54:57], v5 offset:35840\n"
"v_mfma_f32_16x16x32_bf16 v[18:21], a[188:191], v[34:37], v[18:21]\n"
"s_waitcnt lgkmcnt(4)\n"
"v_mfma_f32_16x16x32_bf16 v[22:25], a[172:175], v[38:41], v[22:25]\n"
"v_mfma_f32_16x16x32_bf16 v[26:29], a[180:183], v[38:41], v[26:29]\n"
"v_mfma_f32_16x16x32_bf16 v[30:33], a[188:191], v[38:41], v[30:33]\n"
"s_waitcnt lgkmcnt(3)\n"
"v_mfma_f32_16x16x32_bf16 v[10:13], a[192:195], v[42:45], v[10:13]\n"
"ds_read_b128 v[34:37], v5 offset:36864\n"
"v_mfma_f32_16x16x32_bf16 v[14:17], a[200:203], v[42:45], v[14:17]\n"
"ds_read_b128 v[38:41], v5 offset:37888\n"
"v_mfma_f32_16x16x32_bf16 v[18:21], a[208:211], v[42:45], v[18:21]\n"
"s_waitcnt lgkmcnt(4)\n"
"v_mfma_f32_16x16x32_bf16 v[22:25], a[192:195], v[46:49], v[22:25]\n"
"v_mfma_f32_16x16x32_bf16 v[26:29], a[200:203], v[46:49], v[26:29]\n"
"v_mfma_f32_16x16x32_bf16 v[30:33], a[208:211], v[46:49], v[30:33]\n"
"s_waitcnt lgkmcnt(3)\n"
"v_mfma_f32_16x16x32_bf16 v[10:13], a[196:199], v[50:53], v[10:13]\n"
"ds_read_b128 v[42:45], v5 offset:38912\n"
"v_mfma_f32_16x16x32_bf16 v[14:17], a[204:207], v[50:53], v[14:17]\n"
"ds_read_b128 v[46:49], v5 offset:39936\n"
"v_mfma_f32_16x16x32_bf16 v[18:21], a[212:215], v[50:53], v[18:21]\n"
"s_waitcnt lgkmcnt(4)\n"
"v_mfma_f32_16x16x32_bf16 v[22:25], a[196:199], v[54:57], v[22:25]\n"
"v_mfma_f32_16x16x32_bf16 v[26:29], a[204:207], v[54:57], v[26:29]\n"
"v_mfma_f32_16x16x32_bf16 v[30:33], a[212:215], v[54:57], v[30:33]\n"
"s_waitcnt lgkmcnt(3)\n"
"v_mfma_f32_16x16x32_bf16 v[10:13], a[216:219], v[34:37], v[10:13]\n"
"v_mfma_f32_16x16x32_bf16 v[14:17], a[224:227], v[34:37], v[14:17]\n"
"v_mfma_f32_16x16x32_bf16 v[18:21], a[232:235], v[34:37], v[18:21]\n"
"s_waitcnt lgkmcnt(2)\n"
"v_mfma_f32_16x16x32_bf16 v[22:25], a[216:219], v[38:41], v[22:25]\n"
"v_mfma_f32_16x16x32_bf16 v[26:29], a[224:227], v[38:41], v[26:29]\n"
"v_mfma_f32_16x16x32_bf16 v[30:33], a[232:235], v[38:41], v[30:33]\n"
"s_waitcnt lgkmcnt(1)\n"
"v_mfma_f32_16x16x32_bf16 v[10:13], a[220:223], v[42:45], v[10:13]\n"
"v_mfma_f32_16x16x32_bf16 v[14:17], a[228:231], v[42:45], v[14:17]\n"
"v_mfma_f32_16x16x32_bf16 v[18:21], a[236:239], v[42:45], v[18:21]\n"
"s_waitcnt lgkmcnt(0)\n"
"v_mfma_f32_16x16x32_bf16 v[22:25], a[220:223], v[46:49], v[22:25]\n"
"v_mfma_f32_16x16x32_bf16 v[26:29], a[228:231], v[46:49], v[26:29]\n"
"v_mfma_f32_16x16x32_bf16 v[30:33], a[236:239], v[46:49], v[30:33]\n"
"s_nop 1\n"
"v_cvt_pk_bf16_f32 v64, v10, v11\n"
"v_cvt_pk_bf16_f32 v65, v12, v13\n"
"ds_write_b64 v60, v[64:65] offset:40960\n"
"v_cvt_pk_bf16_f32 v66, v14, v15\n"
"v_cvt_pk_bf16_f32 v67, v16, v17\n"
"ds_write_b64 v61, v[66:67] offset:40960\n"
"v_cvt_pk_bf16_f32 v68, v18, v19\n"
"v_cvt_pk_bf16_f32 v69, v20, v21\n"
"ds_write_b64 v62, v[68:69] offset:40960\n"
"v_cvt_pk_bf16_f32 v70, v22, v23\n"
"v_cvt_pk_bf16_f32 v71, v24, v25\n"
"ds_write_b64 v60, v[70:71] offset:47104\n"
"v_cvt_pk_bf16_f32 v72, v26, v27\n"
"v_cvt_pk_bf16_f32 v73, v28, v29\n"
"ds_write_b64 v61, v[72:73] offset:47104\n"
"v_cvt_pk_bf16_f32 v74, v30, v31\n"
"v_cvt_pk_bf16_f32 v75, v32, v33\n"
"ds_write_b64 v62, v[74:75] offset:47104\n"
"s_waitcnt lgkmcnt(0)\n"
"s_barrier\n"
"ds_read_b128 v[10:13], v5 offset:40960\n"
"ds_read_b128 v[34:37], v5 offset:47104\n"
"ds_read_b128 v[14:17], v5 offset:41984\n"
"ds_read_b128 v[38:41], v5 offset:48128\n"
"s_cmp_ge_u32 s25, 62\n"
"s_cselect_b32 s27, 81920, 0\n"
"s_sub_u32 s96, s96, s27\n"
"s_subb_u32 s97, s97, 0\n"
"s_waitcnt lgkmcnt(3)\n"
"v_mfma_f32_16x16x32_bf16 v[58:61], v[10:13], a[240:243], 0\n"
"ds_read_b128 v[80:83], v4 offset:0\n"
"ds_read_b128 v[18:21], v5 offset:43008\n"
"s_waitcnt lgkmcnt(4)\n"
"v_mfma_f32_16x16x32_bf16 v[62:65], v[34:37], a[240:243], 0\n"
"ds_read_b128 v[84:87], v4 offset:1024\n"
"ds_read_b128 v[42:45], v5 offset:49152\n"
"s_waitcnt lgkmcnt(5)\n"
"v_mfma_f32_16x16x32_bf16 v[58:61], v[14:17], a[244:247], v[58:61]\n"
"ds_read_b128 v[88:91], v4 offset:2048\n"
"ds_read_b128 v[22:25], v5 offset:44032\n"
"s_waitcnt lgkmcnt(6)\n"
"v_mfma_f32_16x16x32_bf16 v[62:65], v[38:41], a[244:247], v[62:65]\n"
"ds_read_b128 v[92:95], v4 offset:3072\n"
"ds_read_b128 v[46:49], v5 offset:50176\n"
"s_waitcnt lgkmcnt(6)\n"
"v_mfma_f32_16x16x32_bf16 v[58:61], v[18:21], a[248:251], v[58:61]\n"
"ds_read_b128 v[96:99], v4 offset:4096\n"
"ds_read_b128 v[26:29], v5 offset:45056\n"
"s_waitcnt lgkmcnt(6)\n"
"v_mfma_f32_16x16x32_bf16 v[62:65], v[42:45], a[248:251], v[62:65]\n"
"ds_read_b128 v[100:103], v4 offset:5120\n"
"ds_read_b128 v[50:53], v5 offset:51200\n"
"s_waitcnt lgkmcnt(6)\n"
"v_mfma_f32_16x16x32_bf16 v[58:61], v[22:25], a[252:255], v[58:61]\n"
"ds_read_b128 v[30:33], v5 offset:46080\n"
"ds_read_b128 v[54:57], v5 offset:52224\n"
"s_waitcnt lgkmcnt(6)\n"
"v_mfma_f32_16x16x32_bf16 v[62:65], v[46:49], a[252:255], v[62:65]\n"
"s_waitcnt lgkmcnt(4)\n"
"v_mfma_f32_16x16x32_bf16 v[58:61], v[26:29], v[104:107], v[58:61]\n"
"s_waitcnt lgkmcnt(2)\n"
"v_mfma_f32_16x16x32_bf16 v[62:65], v[50:53], v[104:107], v[62:65]\n"
"s_waitcnt lgkmcnt(1)\n"
"v_mfma_f32_16x16x32_bf16 v[58:61], v[30:33], v[108:111], v[58:61]\n"
"s_mul_i32 s27, s22, 10240\n"
"s_add_u32 m0, s27, 73728\n"
"s_add_u32 s28, s96, 0\n"
"s_addc_u32 s29, s97, 0\n"
"global_load_lds_dwordx4 v0, s[28:29] offset:0 sc1\n"
"s_waitcnt lgkmcnt(0)\n"
"v_mfma_f32_16x16x32_bf16 v[62:65], v[54:57], v[108:111], v[62:65]\n"
"v_mfma_f32_16x16x32_bf16 v[66:69], v[10:13], v[80:83], 0\n"
"v_mfma_f32_16x16x32_bf16 v[70:73], v[34:37], v[80:83], 0\n"
"v_mfma_f32_16x16x32_bf16 v[66:69], v[14:17], v[84:87], v[66:69]\n"
"v_mfma_f32_16x16x32_bf16 v[70:73], v[38:41], v[84:87], v[70:73]\n"
"v_mfma_f32_16x16x32_bf16 v[66:69], v[18:21], v[88:91], v[66:69]\n"
"v_mfma_f32_16x16x32_bf16 v[70:73], v[42:45], v[88:91], v[70:73]\n"
"v_mfma_f32_16x16x32_bf16 v[66:69], v[22:25], v[92:95], v[66:69]\n"
"v_add_u32 v78, 0x8000, v0\n"
"v_cvt_pk_bf16_f32 v74, v58, v59\n"
"v_mfma_f32_16x16x32_bf16 v[70:73], v[46:49], v[92:95], v[70:73]\n"
"v_cvt_pk_bf16_f32 v75, v60, v61\n"
"v_cvt_pk_bf16_f32 v76, v62, v63\n"
"v_mfma_f32_16x16x32_bf16 v[66:69], v[26:29], v[96:99], v[66:69]\n"
"v_cvt_pk_bf16_f32 v77, v64, v65\n"
"global_store_dwordx4 v78, v[74:77], s[34:35] sc1\n"
"v_mfma_f32_16x16x32_bf16 v[70:73], v[50:53], v[96:99], v[70:73]\n"
"v_mfma_f32_16x16x32_bf16 v[66:69], v[30:33], v[100:103], v[66:69]\n"
"global_load_lds_dwordx4 v0, s[28:29] offset:1024 sc1\n"
"v_mfma_f32_16x16x32_bf16 v[70:73], v[54:57], v[100:103], v[70:73]\n"
"v_mfma_f32_16x16x32_bf16 v[58:61], v[10:13], v[112:115], 0\n"
"ds_read_b128 v[80:83], v4 offset:6144\n"
"v_mfma_f32_16x16x32_bf16 v[62:65], v[34:37], v[112:115], 0\n"
"ds_read_b128 v[84:87], v4 offset:7168\n"
"v_mfma_f32_16x16x32_bf16 v[58:61], v[14:17], v[116:119], v[58:61]\n"
"ds_read_b128 v[88:91], v4 offset:8192\n"
"v_mfma_f32_16x16x32_bf16 v[62:65], v[38:41], v[116:119], v[62:65]\n"
"ds_read_b128 v[92:95], v4 offset:9216\n"
"v_mfma_f32_16x16x32_bf16 v[58:61], v[18:21], v[120:123], v[58:61]\n"
"ds_read_b128 v[96:99], v4 offset:10240\n"
"v_mfma_f32_16x16x32_bf16 v[62:65], v[42:45], v[120:123], v[62:65]\n"
"ds_read_b128 v[100:103], v4 offset:11264\n"
"v_mfma_f32_16x16x32_bf16 v[58:61], v[22:25], v[124:127], v[58:61]\n"
"v_add_u32 v78, 0x0, v0\n"
"v_cvt_pk_bf16_f32 v74, v66, v67\n"
"v_mfma_f32_16x16x32_bf16 v[62:65], v[46:49], v[124:127], v[62:65]\n"
"v_cvt_pk_bf16_f32 v75, v68, v69\n"
"v_cvt_pk_bf16_f32 v76, v70, v71\n"
"v_mfma_f32_16x16x32_bf16 v[58:61], v[26:29], v[128:131], v[58:61]\n"
"v_cvt_pk_bf16_f32 v77, v72, v73\n"
"global_store_dwordx4 v78, v[74:77], s[34:35] sc1\n"
"v_mfma_f32_16x16x32_bf16 v[62:65], v[50:53], v[128:131], v[62:65]\n"
"v_mfma_f32_16x16x32_bf16 v[58:61], v[30:33], v[132:135], v[58:61]\n"
"global_load_lds_dwordx4 v0, s[28:29] offset:2048 sc1\n"
"v_mfma_f32_16x16x32_bf16 v[62:65], v[54:57], v[132:135], v[62:65]\n"
"s_waitcnt lgkmcnt(5)\n"
"v_mfma_f32_16x16x32_bf16 v[66:69], v[10:13], v[80:83], 0\n"
"v_mfma_f32_16x16x32_bf16 v[70:73], v[34:37], v[80:83], 0\n"
"s_waitcnt lgkmcnt(4)\n"
"v_mfma_f32_16x16x32_bf16 v[66:69], v[14:17], v[84:87], v[66:69]\n"
"v_mfma_f32_16x16x32_bf16 v[70:73], v[38:41], v[84:87], v[70:73]\n"
"s_waitcnt lgkmcnt(3)\n"
"v_mfma_f32_16x16x32_bf16 v[66:69], v[18:21], v[88:91], v[66:69]\n"
"v_mfma_f32_16x16x32_bf16 v[70:73], v[42:45], v[88:91], v[70:73]\n"
"s_waitcnt lgkmcnt(2)\n"
"v_mfma_f32_16x16x32_bf16 v[66:69], v[22:25], v[92:95], v[66:69]\n"
"v_add_u32 v78, 0x10000, v0\n"
"v_cvt_pk_bf16_f32 v74, v58, v59\n"
"v_mfma_f32_16x16x32_bf16 v[70:73], v[46:49], v[92:95], v[70:73]\n"
"v_cvt_pk_bf16_f32 v75, v60, v61\n"
"v_cvt_pk_bf16_f32 v76, v62, v63\n"
"s_waitcnt lgkmcnt(1)\n"
"v_mfma_f32_16x16x32_bf16 v[66:69], v[26:29], v[96:99], v[66:69]\n"
"v_cvt_pk_bf16_f32 v77, v64, v65\n"
"global_store_dwordx4 v78, v[74:77], s[34:35] sc1\n"
"v_mfma_f32_16x16x32_bf16 v[70:73], v[50:53], v[96:99], v[70:73]\n"
"s_waitcnt lgkmcnt(0)\n"
"v_mfma_f32_16x16x32_bf16 v[66:69], v[30:33], v[100:103], v[66:69]\n"
"global_load_lds_dwordx4 v0, s[28:29] offset:3072 sc1\n"
"v_mfma_f32_16x16x32_bf16 v[70:73], v[54:57], v[100:103], v[70:73]\n"
"v_mfma_f32_16x16x32_bf16 v[58:61], v[10:13], v[136:139], 0\n"
"ds_read_b128 v[80:83], v4 offset:12288\n"
"v_mfma_f32_16x16x32_bf16 v[62:65], v[34:37], v[136:139], 0\n"
"ds_read_b128 v[84:87], v4 offset:13312\n"
"v_mfma_f32_16x16x32_bf16 v[58:61], v[14:17], v[140:143], v[58:61]\n"
"ds_read_b128 v[88:91], v4 offset:14336\n"
"v_mfma_f32_16x16x32_bf16 v[62:65], v[38:41], v[140:143], v[62:65]\n"
"ds_read_b128 v[92:95], v4 offset:15360\n"
"v_mfma_f32_16x16x32_bf16 v[58:61], v[18:21], v[144:147], v[58:61]\n"
"ds_read_b128 v[96:99], v4 offset:16384\n"
"v_mfma_f32_16x16x32_bf16 v[62:65], v[42:45], v[144:147], v[62:65]\n"
"ds_read_b128 v[100:103], v4 offset:17408\n"
"v_mfma_f32_16x16x32_bf16 v[58:61], v[22:25], v[148:151], v[58:61]\n"
"v_add_u32 v78, 0x20000, v0\n"
"v_cvt_pk_bf16_f32 v74, v66, v67\n"
"v_mfma_f32_16x16x32_bf16 v[62:65], v[46:49], v[148:151], v[62:65]\n"
"v_cvt_pk_bf16_f32 v75, v68, v69\n"
"v_cvt_pk_bf16_f32 v76, v70, v71\n"
"v_mfma_f32_16x16x32_bf16 v[58:61], v[26:29], v[152:155], v[58:61]\n"
"v_cvt_pk_bf16_f32 v77, v72, v73\n"
"global_store_dwordx4 v78, v[74:77], s[34:35] sc1\n"
"v_mfma_f32_16x16x32_bf16 v[62:65], v[50:53], v[152:155], v[62:65]\n"
"v_mfma_f32_16x16x32_bf16 v[58:61], v[30:33], v[156:159], v[58:61]\n"
"s_mul_i32 s27, s22, 10240\n"
"s_add_u32 m0, s27, 77824\n"
"s_add_u32 s28, s96, 4096\n"
"s_addc_u32 s29, s97, 0\n"
"global_load_lds_dwordx4 v0, s[28:29] offset:0 sc1\n"
"v_mfma_f32_16x16x32_bf16 v[62:65], v[54:57], v[156:159], v[62:65]\n"
"s_waitcnt lgkmcnt(5)\n"
"v_mfma_f32_16x16x32_bf16 v[66:69], v[10:13], v[80:83], 0\n"
"v_mfma_f32_16x16x32_bf16 v[70:73], v[34:37], v[80:83], 0\n"
"s_waitcnt lgkmcnt(4)\n"
"v_mfma_f32_16x16x32_bf16 v[66:69], v[14:17], v[84:87], v[66:69]\n"
"v_mfma_f32_16x16x32_bf16 v[70:73], v[38:41], v[84:87], v[70:73]\n"
"s_waitcnt lgkmcnt(3)\n"
"v_mfma_f32_16x16x32_bf16 v[66:69], v[18:21], v[88:91], v[66:69]\n"
"v_mfma_f32_16x16x32_bf16 v[70:73], v[42:45], v[88:91], v[70:73]\n"
"s_waitcnt lgkmcnt(2)\n"
"v_mfma_f32_16x16x32_bf16 v[66:69], v[22:25], v[92:95], v[66:69]\n"
"v_add_u32 v78, 0x18000, v0\n"
"v_cvt_pk_bf16_f32 v74, v58, v59\n"
"v_mfma_f32_16x16x32_bf16 v[70:73], v[46:49], v[92:95], v[70:73]\n"
"v_cvt_pk_bf16_f32 v75, v60, v61\n"
"v_cvt_pk_bf16_f32 v76, v62, v63\n"
"s_waitcnt lgkmcnt(1)\n"
"v_mfma_f32_16x16x32_bf16 v[66:69], v[26:29], v[96:99], v[66:69]\n"
"v_cvt_pk_bf16_f32 v77, v64, v65\n"
"global_store_dwordx4 v78, v[74:77], s[34:35] sc1\n"
"v_mfma_f32_16x16x32_bf16 v[70:73], v[50:53], v[96:99], v[70:73]\n"
"s_waitcnt lgkmcnt(0)\n"
"v_mfma_f32_16x16x32_bf16 v[66:69], v[30:33], v[100:103], v[66:69]\n"
"global_load_lds_dwordx4 v0, s[28:29] offset:1024 sc1\n"
"v_mfma_f32_16x16x32_bf16 v[70:73], v[54:57], v[100:103], v[70:73]\n"
"v_mfma_f32_16x16x32_bf16 v[58:61], v[10:13], v[160:163], 0\n"
"v_mfma_f32_16x16x32_bf16 v[62:65], v[34:37], v[160:163], 0\n"
"v_mfma_f32_16x16x32_bf16 v[58:61], v[14:17], v[164:167], v[58:61]\n"
"v_mfma_f32_16x16x32_bf16 v[62:65], v[38:41], v[164:167], v[62:65]\n"
"v_mfma_f32_16x16x32_bf16 v[58:61], v[18:21], v[168:171], v[58:61]\n"
"v_mfma_f32_16x16x32_bf16 v[62:65], v[42:45], v[168:171], v[62:65]\n"
"v_mfma_f32_16x16x32_bf16 v[58:61], v[22:25], v[172:175], v[58:61]\n"
"v_add_u32 v78, 0x40000, v0\n"
"v_cvt_pk_bf16_f32 v74, v66, v67\n"
"v_mfma_f32_16x16x32_bf16 v[62:65], v[46:49], v[172:175], v[62:65]\n"
"v_cvt_pk_bf16_f32 v75, v68, v69\n"
"v_cvt_pk_bf16_f32 v76, v70, v71\n"
"v_mfma_f32_16x16x32_bf16 v[58:61], v[26:29], v[176:179], v[58:61]\n"
"v_cvt_pk_bf16_f32 v77, v72, v73\n"
"global_store_dwordx4 v78, v[74:77], s[34:35] sc1\n"
"v_mfma_f32_16x16x32_bf16 v[62:65], v[50:53], v[176:179], v[62:65]\n"
"v_mfma_f32_16x16x32_bf16 v[58:61], v[30:33], v[180:183], v[58:61]\n"
"global_load_lds_dwordx4 v0, s[28:29] offset:2048 sc1\n"
"v_mfma_f32_16x16x32_bf16 v[62:65], v[54:57], v[180:183], v[62:65]\n"
"v_mfma_f32_16x16x32_bf16 v[66:69], v[10:13], v[184:187], 0\n"
"v_mfma_f32_16x16x32_bf16 v[70:73], v[34:37], v[184:187], 0\n"
"v_mfma_f32_16x16x32_bf16 v[66:69], v[14:17], v[188:191], v[66:69]\n"
"v_mfma_f32_16x16x32_bf16 v[70:73], v[38:41], v[188:191], v[70:73]\n"
"v_mfma_f32_16x16x32_bf16 v[66:69], v[18:21], v[192:195], v[66:69]\n"
"v_mfma_f32_16x16x32_bf16 v[70:73], v[42:45], v[192:195], v[70:73]\n"
"v_mfma_f32_16x16x32_bf16 v[66:69], v[22:25], v[196:199], v[66:69]\n"
"v_add_u32 v78, 0x28000, v0\n"
"v_cvt_pk_bf16_f32 v74, v58, v59\n"
"v_mfma_f32_16x16x32_bf16 v[70:73], v[46:49], v[196:199], v[70:73]\n"
"v_cvt_pk_bf16_f32 v75, v60, v61\n"
"v_cvt_pk_bf16_f32 v76, v62, v63\n"
"v_mfma_f32_16x16x32_bf16 v[66:69], v[26:29], v[200:203], v[66:69]\n"
"v_cvt_pk_bf16_f32 v77, v64, v65\n"
"global_store_dwordx4 v78, v[74:77], s[34:35] sc1\n"
"v_mfma_f32_16x16x32_bf16 v[70:73], v[50:53], v[200:203], v[70:73]\n"
"v_mfma_f32_16x16x32_bf16 v[66:69], v[30:33], v[204:207], v[66:69]\n"
"global_load_lds_dwordx4 v0, s[28:29] offset:3072 sc1\n"
"v_mfma_f32_16x16x32_bf16 v[70:73], v[54:57], v[204:207], v[70:73]\n"
"v_mfma_f32_16x16x32_bf16 v[58:61], v[10:13], v[208:211], 0\n"
"v_mfma_f32_16x16x32_bf16 v[62:65], v[34:37], v[208:211], 0\n"
"v_mfma_f32_16x16x32_bf16 v[58:61], v[14:17], v[212:215], v[58:61]\n"
"v_mfma_f32_16x16x32_bf16 v[62:65], v[38:41], v[212:215], v[62:65]\n"
"v_mfma_f32_16x16x32_bf16 v[58:61], v[18:21], v[216:219], v[58:61]\n"
"v_mfma_f32_16x16x32_bf16 v[62:65], v[42:45], v[216:219], v[62:65]\n"
"v_mfma_f32_16x16x32_bf16 v[58:61], v[22:25], v[220:223], v[58:61]\n"
"v_add_u32 v78, 0x30000, v0\n"
"v_cvt_pk_bf16_f32 v74, v66, v67\n"
"v_mfma_f32_16x16x32_bf16 v[62:65], v[46:49], v[220:223], v[62:65]\n"
"v_cvt_pk_bf16_f32 v75, v68, v69\n"
"v_cvt_pk_bf16_f32 v76, v70, v71\n"
"v_mfma_f32_16x16x32_bf16 v[58:61], v[26:29], v[224:227], v[58:61]\n"
"v_cvt_pk_bf16_f32 v77, v72, v73\n"
"global_store_dwordx4 v78, v[74:77], s[34:35] sc1\n"
"v_mfma_f32_16x16x32_bf16 v[62:65], v[50:53], v[224:227], v[62:65]\n"
"v_mfma_f32_16x16x32_bf16 v[58:61], v[30:33], v[228:231], v[58:61]\n"
"s_mul_i32 s27, s22, 10240\n"
"s_add_u32 m0, s27, 81920\n"
"s_add_u32 s28, s96, 8192\n"
"s_addc_u32 s29, s97, 0\n"
"global_load_lds_dwordx4 v0, s[28:29] offset:0 sc1\n"
"v_mfma_f32_16x16x32_bf16 v[62:65], v[54:57], v[228:231], v[62:65]\n"
"v_mfma_f32_16x16x32_bf16 v[66:69], v[10:13], v[232:235], 0\n"
"v_mfma_f32_16x16x32_bf16 v[70:73], v[34:37], v[232:235], 0\n"
"v_mfma_f32_16x16x32_bf16 v[66:69], v[14:17], v[236:239], v[66:69]\n"
"v_mfma_f32_16x16x32_bf16 v[70:73], v[38:41], v[236:239], v[70:73]\n"
"v_mfma_f32_16x16x32_bf16 v[66:69], v[18:21], v[240:243], v[66:69]\n"
"v_mfma_f32_16x16x32_bf16 v[70:73], v[42:45], v[240:243], v[70:73]\n"
"v_mfma_f32_16x16x32_bf16 v[66:69], v[22:25], v[244:247], v[66:69]\n"
"v_add_u32 v78, 0x38000, v0\n"
"v_cvt_pk_bf16_f32 v74, v58, v59\n"
"v_mfma_f32_16x16x32_bf16 v[70:73], v[46:49], v[244:247], v[70:73]\n"
"v_cvt_pk_bf16_f32 v75, v60, v61\n"
"v_cvt_pk_bf16_f32 v76, v62, v63\n"
"v_mfma_f32_16x16x32_bf16 v[66:69], v[26:29], v[248:251], v[66:69]\n"
"v_cvt_pk_bf16_f32 v77, v64, v65\n"
"global_store_dwordx4 v78, v[74:77], s[34:35] sc1\n"
"v_mfma_f32_16x16x32_bf16 v[70:73], v[50:53], v[248:251], v[70:73]\n"
"v_mfma_f32_16x16x32_bf16 v[66:69], v[30:33], v[252:255], v[66:69]\n"
"global_load_lds_dwordx4 v0, s[28:29] offset:1024 sc1\n"
"v_mfma_f32_16x16x32_bf16 v[70:73], v[54:57], v[252:255], v[70:73]\n"
"v_add_u32 v78, 0x48000, v0\n"
"s_nop 4\n"
"v_cvt_pk_bf16_f32 v74, v66, v67\n"
"v_cvt_pk_bf16_f32 v75, v68, v69\n"
"v_cvt_pk_bf16_f32 v76, v70, v71\n"
"v_cvt_pk_bf16_f32 v77, v72, v73\n"
"global_store_dwordx4 v78, v[74:77], s[34:35] sc1\n"
"s_add_u32 s96, s96, 0x14000\n"
"s_addc_u32 s97, s97, 0\n"
"ds_read_b128 v[78:81], v4 offset:0\n"
"ds_read_b128 v[82:85], v4 offset:1024\n"
"ds_read_b128 v[86:89], v4 offset:2048\n"
"ds_read_b128 v[90:93], v4 offset:3072\n"
"ds_read_b128 v[94:97], v4 offset:4096\n"
"ds_read_b128 v[98:101], v4 offset:5120\n"
"v_mov_b64 v[50:51], 0\n"
"v_mov_b64 v[52:53], 0\n"
"v_mov_b64 v[54:55], 0\n"
"v_mov_b64 v[56:57], 0\n"
"v_mov_b64 v[58:59], 0\n"
"v_mov_b64 v[60:61], 0\n"
"v_mov_b64 v[62:63], 0\n"
"v_mov_b64 v[64:65], 0\n"
"v_mov_b64 v[66:67], 0\n"
"v_mov_b64 v[68:69], 0\n"
"v_mov_b64 v[70:71], 0\n"
"v_mov_b64 v[72:73], 0\n"
"v_mov_b64 v[74:75], 0\n"
"v_mov_b64 v[76:77], 0\n"
"s_lshl_b32 s27, s22, 11\n"
"s_min_u32 s27, s27, 5120\n"
"s_add_u32 s28, s27, 0x1f000\n"
"v_add_u32 v9, s28, v0\n"
"s_add_u32 s27, s27, 3072\n"
"s_cmp_ge_u32 s27, 6144\n"
"s_cselect_b32 s28, 10240, 0\n"
"s_add_u32 s28, s27, s28\n"
"s_add_u32 s66, s30, s28\n"
"s_addc_u32 s67, s31, 0\n"
"s_add_u32 s27, s27, 1024\n"
"s_cmp_ge_u32 s27, 6144\n"
"s_cselect_b32 s28, 10240, 0\n"
"s_add_u32 s27, s27, s28\n"
"s_add_u32 s28, s30, s27\n"
"s_addc_u32 s29, s31, 0\n"
"s_add_u32 s70, s25, 1\n"
"s_waitcnt vmcnt(0)\n"
"s_mov_b64 exec, 1\n"
"s_add_u32 s27, s25, 1\n"
"v_mov_b32 v28, s27\n"
"v_mov_b32 v29, 0\n"
"global_store_dword v29, v28, s[38:39] sc1\n"
"s_mov_b64 exec, -1\n"
"s_cmp_lg_u32 s22, 0\n"
"s_cbranch_scc1 .Lv_tkok_5\n"
"v_mov_b32 v25, 0\n"
"global_load_dword v22, v25, s[98:99] sc1\n"
".Lv_tk_4:\n"
"global_load_dword v23, v25, s[98:99] sc1\n"
"s_waitcnt vmcnt(1)\n"
"v_cmp_gt_u32 vcc, s70, v22\n"
"s_cbranch_vccz .Lv_tkok_5\n"
"global_load_dword v22, v25, s[98:99] sc1\n"
"s_waitcnt vmcnt(1)\n"
"v_cmp_gt_u32 vcc, s70, v23\n"
"s_cbranch_vccz .Lv_tkok_5\n"
"s_add_u32 s26, s26, 1\n"
"s_cmp_lt_u32 s26, 0x80000\n"
"s_cbranch_scc1 .Lv_tk_4\n"
"s_branch .Lv_abort\n"
".Lv_tkok_5:\n"
"s_barrier\n"
"global_load_dwordx4 v[10:13], v0, s[30:31] sc1\n"
"s_nop 9\n"
"global_load_dwordx4 v[42:45], v0, s[66:67] sc1\n"
"s_nop 9\n"
"global_load_dwordx4 v[46:49], v0, s[28:29] sc1\n"
"s_nop 9\n"
"global_load_dwordx4 v[14:17], v0, s[30:31] offset:1024 sc1\n"
"s_nop 9\n"
"global_load_dwordx4 v[18:21], v0, s[30:31] offset:2048 sc1\n"
".Lv_ww_6:\n"
"v_mov_b32 v102, v102\n"
"s_getreg_b32 s27, hwreg(HW_REG_IB_STS, 0, 4)\n"
"s_cmp_gt_u32 s27, 4\n"
"s_cbranch_scc1 .Lv_ww_6\n"
"s_waitcnt vmcnt(4)\n"
"v_smfmac_f32_16x16x64_bf16 v[50:53], v[10:13], a[0:7], v1\n"
"v_max_u32 v102, v10, v12\n"
"v_smfmac_f32_16x16x64_bf16 v[54:57], v[10:13], a[8:15], v1\n"
"s_waitcnt vmcnt(3)\n"
"ds_write_b128 v9, v[42:45]\n"
"v_smfmac_f32_16x16x64_bf16 v[58:61], v[10:13], a[16:23], v1\n"
"s_waitcnt vmcnt(2)\n"
"ds_write_b128 v9, v[46:49] offset:1024\n"
"s_waitcnt vmcnt(1)\n"
"v_smfmac_f32_16x16x64_bf16 v[50:53], v[14:17], a[24:31], v1\n"
"v_max3_u32 v102, v102, v14, v16\n"
"v_smfmac_f32_16x16x64_bf16 v[54:57], v[14:17], a[32:39], v1\n"
"v_smfmac_f32_16x16x64_bf16 v[58:61], v[14:17], a[40:47], v1\n"
"s_waitcnt vmcnt(0)\n"
"v_smfmac_f32_16x16x64_bf16 v[50:53], v[18:21], a[48:55], v1\n"
"v_max3_u32 v102, v102, v18, v20\n"
"v_smfmac_f32_16x16x64_bf16 v[54:57], v[18:21], a[56:63], v1\n"
"s_waitcnt lgkmcnt(0)\n"
"s_barrier\n"
"ds_read_b128 v[22:25], v6 offset:0\n"
"ds_read_b128 v[26:29], v6 offset:1024\n"
"v_smfmac_f32_16x16x64_bf16 v[58:61], v[18:21], a[64:71], v1\n"
"ds_read_b128 v[30:33], v6 offset:2048\n"
"ds_read_b128 v[34:37], v6 offset:3072\n"
"s_waitcnt lgkmcnt(3)\n"
"v_smfmac_f32_16x16x64_bf16 v[50:53], v[22:25], a[72:79], v1\n"
"ds_read_b128 v[38:41], v6 offset:4096\n"
"ds_read_b128 v[42:45], v6 offset:5120\n"
"v_smfmac_f32_16x16x64_bf16 v[54:57], v[22:25], a[80:87], v1\n"
"ds_read_b128 v[46:49], v6 offset:6144\n"
"v_max3_u32 v102, v102, v22, v24\n"
"v_smfmac_f32_16x16x64_bf16 v[58:61], v[22:25], a[88:95], v1\n"
"s_waitcnt lgkmcnt(5)\n"
"v_max3_u32 v102, v102, v26, v28\n"
"s_waitcnt lgkmcnt(4)\n"
"v_max3_u32 v102, v102, v30, v32\n"
"v_smfmac_f32_16x16x64_bf16 v[50:53], v[26:29], a[96:103], v1\n"
"s_waitcnt lgkmcnt(3)\n"
"v_max3_u32 v102, v102, v34, v36\n"
"s_waitcnt lgkmcnt(2)\n"
"v_max3_u32 v102, v102, v38, v40\n"
"v_smfmac_f32_16x16x64_bf16 v[54:57], v[26:29], a[104:111], v1\n"
"s_waitcnt lgkmcnt(1)\n"
"v_max3_u32 v102, v102, v42, v44\n"
"s_waitcnt lgkmcnt(0)\n"
"v_max3_u32 v102, v102, v46, v48\n"
"v_smfmac_f32_16x16x64_bf16 v[58:61], v[26:29], a[112:119], v1\n"
"v_smfmac_f32_16x16x64_bf16 v[50:53], v[30:33], a[120:127], v1\n"
"v_smfmac_f32_16x16x64_bf16 v[54:57], v[30:33], a[128:135], v1\n"
"v_smfmac_f32_16x16x64_bf16 v[58:61], v[30:33], a[136:143], v1\n"
"v_cmp_eq_u32 vcc, -1, v102\n"
"s_cbranch_vccnz .Lv_mfail_7\n"
".Lv_mtail_8:\n"
"v_smfmac_f32_16x16x64_bf16 v[50:53], v[34:37], a[144:151], v1\n"
"v_mov_b64 v[10:11], 0\n"
"v_mov_b64 v[12:13], 0\n"
"v_smfmac_f32_16x16x64_bf16 v[50:53], v[38:41], a[168:175], v1\n"
"v_mov_b64 v[14:15], 0\n"
"v_mov_b64 v[16:17], 0\n"
"v_smfmac_f32_16x16x64_bf16 v[50:53], v[42:45], a[192:199], v1\n"
"v_mov_b64 v[18:19], 0\n"
"v_mov_b64 v[20:21], 0\n"
"v_smfmac_f32_16x16x64_bf16 v[50:53], v[46:49], a[216:223], v1\n"
"v_mov_b64 v[22:23], 0\n"
"v_mov_b64 v[24:25], 0\n"
"v_smfmac_f32_16x16x64_bf16 v[54:57], v[34:37], a[152:159], v1\n"
"v_mov_b64 v[26:27], 0\n"
"v_mov_b64 v[28:29], 0\n"
"v_smfmac_f32_16x16x64_bf16 v[54:57], v[38:41], a[176:183], v1\n"
"v_mov_b64 v[30:31], 0\n"
"v_mov_b64 v[32:33], 0\n"
"v_smfmac_f32_16x16x64_bf16 v[54:57], v[42:45], a[200:207], v1\n"
"v_smfmac_f32_16x16x64_bf16 v[54:57], v[46:49], a[224:231], v1\n"
"v_add_f32 v50, v50, v51\n"
"v_add_f32 v52, v52, v53\n"
"v_smfmac_f32_16x16x64_bf16 v[58:61], v[34:37], a[160:167], v1\n"
"v_cvt_pk_bf16_f32 v50, v50, v52\n"
"ds_write_b16 v2, v50 offset:0\n"
"v_smfmac_f32_16x16x64_bf16 v[58:61], v[38:41], a[184:191], v1\n"
"ds_write_b16_d16_hi v2, v50 offset:4\n"
"v_smfmac_f32_16x16x64_bf16 v[58:61], v[42:45], a[208:215], v1\n"
"v_smfmac_f32_16x16x64_bf16 v[58:61], v[46:49], a[232:239], v1\n"
"v_add_f32 v54, v54, v55\n"
"v_add_f32 v56, v56, v57\n"
"v_cvt_pk_bf16_f32 v54, v54, v56\n"
"ds_write_b16 v2, v54 offset:256\n"
"ds_write_b16_d16_hi v2, v54 offset:260\n"
"s_nop 2\n"
"v_add_f32 v58, v58, v59\n"
"v_add_f32 v60, v60, v61\n"
"v_cvt_pk_bf16_f32 v58, v58, v60\n"
"ds_write_b16 v2, v58 offset:512\n"
"ds_write_b16_d16_hi v2, v58 offset:516\n"
"ds_read_b128 v[34:37], v4 offset:6144\n"
"ds_read_b128 v[38:41], v4 offset:7168\n"
"ds_read_b128 v[42:45], v4 offset:8192\n"
"ds_read_b128 v[46:49], v4 offset:9216\n"
"s_waitcnt lgkmcnt(4)\n"
"s_barrier\n"
"ds_read_b64_tr_b16 v[50:51], v3 offset:0\n"
"ds_read_b64_tr_b16 v[52:53], v3 offset:64\n"
"ds_read_b64_tr_b16 v[54:55], v3 offset:1024\n"
"ds_read_b64_tr_b16 v[56:57], v3 offset:1088\n"
"ds_read_b64_tr_b16 v[58:59], v3 offset:2048\n"
"ds_read_b64_tr_b16 v[60:61], v3 offset:2112\n"
"s_waitcnt lgkmcnt(4)\n"
"v_smfmac_f32_16x16x64_bf16 v[62:65], v[50:53], v[78:85], v1\n"
"s_waitcnt lgkmcnt(2)\n"
"v_smfmac_f32_16x16x64_bf16 v[62:65], v[54:57], v[86:93], v1\n"
"s_waitcnt lgkmcnt(0)\n"
"v_smfmac_f32_16x16x64_bf16 v[62:65], v[58:61], v[94:101], v1\n"
"v_smfmac_f32_16x16x64_bf16 v[66:69], v[50:53], a[240:247], v1\n"
"ds_read_b128 v[78:81], v4 offset:10240\n"
"s_add_u32 s28, s36, 0x10000\n"
"v_smfmac_f32_16x16x64_bf16 v[70:73], v[50:53], v[112:119], v1\n"
"ds_read_b128 v[82:85], v4 offset:11264\n"
"s_addc_u32 s29, s37, 0\n"
"v_smfmac_f32_16x16x64_bf16 v[74:77], v[50:53], v[136:143], v1\n"
"ds_read_b128 v[86:89], v4 offset:12288\n"
"v_smfmac_f32_16x16x64_bf16 v[66:69], v[54:57], a[248:255], v1\n"
"ds_read_b128 v[90:93], v4 offset:13312\n"
"v_smfmac_f32_16x16x64_bf16 v[70:73], v[54:57], v[120:127], v1\n"
"ds_read_b128 v[94:97], v4 offset:14336\n"
"v_smfmac_f32_16x16x64_bf16 v[74:77], v[54:57], v[144:151], v1\n"
"ds_read_b128 v[98:101], v4 offset:15360\n"
"v_smfmac_f32_16x16x64_bf16 v[66:69], v[58:61], v[104:111], v1\n"
"v_smfmac_f32_16x16x64_bf16 v[70:73], v[58:61], v[128:135], v1\n"
"v_smfmac_f32_16x16x64_bf16 v[74:77], v[58:61], v[152:159], v1\n"
"v_smfmac_f32_16x16x64_bf16 v[10:13], v[50:53], v[34:41], v1\n"
"v_smfmac_f32_16x16x64_bf16 v[10:13], v[54:57], v[42:49], v1\n"
"v_add_f32 v62, v62, v63\n"
"v_add_f32 v64, v64, v65\n"
"ds_read_b128 v[34:37], v4 offset:16384\n"
"s_waitcnt lgkmcnt(5)\n"
"v_smfmac_f32_16x16x64_bf16 v[10:13], v[58:61], v[78:85], v1\n"
"v_cvt_pk_bf16_f32 v62, v62, v64\n"
"v_add_f32 v66, v66, v67\n"
"ds_read_b128 v[38:41], v4 offset:17408\n"
"v_smfmac_f32_16x16x64_bf16 v[14:17], v[50:53], v[160:167], v1\n"
"v_add_f32 v68, v68, v69\n"
"v_cvt_pk_bf16_f32 v63, v66, v68\n"
"v_smfmac_f32_16x16x64_bf16 v[18:21], v[50:53], v[184:191], v1\n"
"v_add_f32 v70, v70, v71\n"
"v_add_f32 v72, v72, v73\n"
"v_smfmac_f32_16x16x64_bf16 v[22:25], v[50:53], v[208:215], v1\n"
"v_cvt_pk_bf16_f32 v64, v70, v72\n"
"v_add_f32 v74, v74, v75\n"
"v_smfmac_f32_16x16x64_bf16 v[14:17], v[54:57], v[168:175], v1\n"
"v_add_f32 v76, v76, v77\n"
"v_cvt_pk_bf16_f32 v65, v74, v76\n"
"v_smfmac_f32_16x16x64_bf16 v[18:21], v[54:57], v[192:199], v1\n"
"v_permlane32_swap_b32 v62, v64\n"
"v_permlane32_swap_b32 v63, v65\n"
"v_smfmac_f32_16x16x64_bf16 v[22:25], v[54:57], v[216:223], v1\n"
"s_nop 0\n"
"v_permlane16_swap_b32 v62, v63\n"
"v_permlane16_swap_b32 v64, v65\n"
"v_smfmac_f32_16x16x64_bf16 v[14:17], v[58:61], v[176:183], v1\n"
"global_store_dwordx4 v7, v[62:65], s[36:37] sc1\n"
"v_smfmac_f32_16x16x64_bf16 v[18:21], v[58:61], v[200:207], v1\n"
"v_smfmac_f32_16x16x64_bf16 v[22:25], v[58:61], v[224:231], v1\n"
"s_waitcnt lgkmcnt(4)\n"
"v_smfmac_f32_16x16x64_bf16 v[26:29], v[50:53], v[86:93], v1\n"
"v_smfmac_f32_16x16x64_bf16 v[30:33], v[50:53], v[232:239], v1\n"
"v_add_f32 v10, v10, v11\n"
"v_add_f32 v12, v12, v13\n"
"s_waitcnt lgkmcnt(2)\n"
"v_smfmac_f32_16x16x64_bf16 v[26:29], v[54:57], v[94:101], v1\n"
"v_cvt_pk_bf16_f32 v10, v10, v12\n"
"v_add_f32 v14, v14, v15\n"
"v_smfmac_f32_16x16x64_bf16 v[30:33], v[54:57], v[240:247], v1\n"
"v_add_f32 v16, v16, v17\n"
"v_cvt_pk_bf16_f32 v11, v14, v16\n"
"s_waitcnt lgkmcnt(0)\n"
"v_smfmac_f32_16x16x64_bf16 v[26:29], v[58:61], v[34:41], v1\n"
"v_add_f32 v18, v18, v19\n"
"v_add_f32 v20, v20, v21\n"
"v_smfmac_f32_16x16x64_bf16 v[30:33], v[58:61], v[248:255], v1\n"
"v_cvt_pk_bf16_f32 v12, v18, v20\n"
"v_add_f32 v22, v22, v23\n"
"v_add_f32 v24, v24, v25\n"
"v_cvt_pk_bf16_f32 v13, v22, v24\n"
"v_permlane32_swap_b32 v10, v12\n"
"s_nop 0\n"
"v_permlane32_swap_b32 v11, v13\n"
"s_nop 1\n"
"v_permlane16_swap_b32 v10, v11\n"
"v_permlane16_swap_b32 v12, v13\n"
"global_store_dwordx4 v7, v[10:13], s[28:29] sc1\n"
"s_add_u32 s28, s36, 0x20000\n"
"s_addc_u32 s29, s37, 0\n"
"v_add_f32 v26, v26, v27\n"
"v_add_f32 v28, v28, v29\n"
"v_cvt_pk_bf16_f32 v26, v26, v28\n"
"v_add_f32 v30, v30, v31\n"
"v_add_f32 v32, v32, v33\n"
"v_cvt_pk_bf16_f32 v27, v30, v32\n"
"v_mov_b64 v[28:29], v[26:27]\n"
"s_nop 1\n"
"v_permlane32_swap_b32 v26, v28\n"
"v_permlane32_swap_b32 v27, v29\n"
"s_nop 1\n"
"v_permlane16_swap_b32 v26, v27\n"
"v_permlane16_swap_b32 v28, v29\n"
"s_mov_b64 exec, s[40:41]\n"
"global_store_dwordx4 v7, v[26:29], s[28:29] sc1\n"
"s_mov_b64 exec, -1\n"
"s_add_u32 s56, s56, 0x8000\n"
"s_addc_u32 s57, s57, 0\n"
"s_mov_b64 s[30:31], s[56:57]\n"
"s_mov_b64 s[28:29], s[34:35]\n"
"s_mov_b64 s[34:35], s[60:61]\n"
"s_mov_b64 s[60:61], s[28:29]\n"
"s_mov_b64 s[28:29], s[36:37]\n"
"s_mov_b64 s[36:37], s[62:63]\n"
"s_mov_b64 s[62:63], s[28:29]\n"
"s_add_u32 s25, s25, 2\n"
"s_cmp_lt_u32 s25, 64\n"
"s_cbranch_scc1 .Lv_top_3\n"
"s_branch .Lv_end\n"
".Lv_mfail_7:\n"
"s_waitcnt vmcnt(0) lgkmcnt(0)\n"
"s_nop 7\n"
"s_nop 7\n"
"v_mov_b64 v[50:51], 0\n"
"v_mov_b64 v[52:53], 0\n"
"v_mov_b64 v[54:55], 0\n"
"v_mov_b64 v[56:57], 0\n"
"v_mov_b64 v[58:59], 0\n"
"v_mov_b64 v[60:61], 0\n"
"s_add_u32 s28, s30, 0x4000\n"
"s_addc_u32 s29, s31, 0\n"
"s_add_u32 s30, s30, 0x800\n"
"s_addc_u32 s31, s31, 0\n"
".Lv_mfl_9:\n"
"global_load_dwordx4 v[10:13], v0, s[30:31] offset:-2048 sc1\n"
"global_load_dwordx4 v[14:17], v0, s[30:31] offset:-1024 sc1\n"
"global_load_dwordx4 v[18:21], v0, s[30:31] offset:0 sc1\n"
"global_load_dwordx4 v[22:25], v0, s[30:31] offset:1024 sc1\n"
"global_load_dwordx4 v[26:29], v0, s[30:31] offset:2048 sc1\n"
"global_load_dwordx4 v[30:33], v0, s[30:31] offset:3072 sc1\n"
"global_load_dwordx4 v[34:37], v0, s[28:29] offset:0 sc1\n"
"global_load_dwordx4 v[38:41], v0, s[28:29] offset:1024 sc1\n"
"global_load_dwordx4 v[42:45], v0, s[28:29] offset:2048 sc1\n"
"global_load_dwordx4 v[46:49], v0, s[28:29] offset:3072 sc1\n"
"s_waitcnt vmcnt(9)\n"
"v_max_u32 v102, v10, v12\n"
"s_waitcnt vmcnt(8)\n"
"v_max3_u32 v102, v102, v14, v16\n"
"s_waitcnt vmcnt(7)\n"
"v_max3_u32 v102, v102, v18, v20\n"
"s_waitcnt vmcnt(6)\n"
"v_max3_u32 v102, v102, v22, v24\n"
"s_waitcnt vmcnt(5)\n"
"v_max3_u32 v102, v102, v26, v28\n"
"s_waitcnt vmcnt(4)\n"
"v_max3_u32 v102, v102, v30, v32\n"
"s_waitcnt vmcnt(3)\n"
"v_max3_u32 v102, v102, v34, v36\n"
"s_waitcnt vmcnt(2)\n"
"v_max3_u32 v102, v102, v38, v40\n"
"s_waitcnt vmcnt(1)\n"
"v_max3_u32 v102, v102, v42, v44\n"
"s_waitcnt vmcnt(0)\n"
"v_max3_u32 v102, v102, v46, v48\n"
"v_cmp_eq_u32 vcc, -1, v102\n"
"s_cbranch_vccz .Lv_mfo_10\n"
"s_add_u32 s26, s26, 1\n"
"s_cmp_lt_u32 s26, 0x80000\n"
"s_cbranch_scc1 .Lv_mfl_9\n"
"s_branch .Lv_abort\n"
".Lv_mfo_10:\n"
"v_smfmac_f32_16x16x64_bf16 v[50:53], v[10:13], a[0:7], v1\n"
"v_smfmac_f32_16x16x64_bf16 v[54:57], v[10:13], a[8:15], v1\n"
"v_smfmac_f32_16x16x64_bf16 v[58:61], v[10:13], a[16:23], v1\n"
"v_smfmac_f32_16x16x64_bf16 v[50:53], v[14:17], a[24:31], v1\n"
"v_smfmac_f32_16x16x64_bf16 v[54:57], v[14:17], a[32:39], v1\n"
"v_smfmac_f32_16x16x64_bf16 v[58:61], v[14:17], a[40:47], v1\n"
"v_smfmac_f32_16x16x64_bf16 v[50:53], v[18:21], a[48:55], v1\n"
"v_smfmac_f32_16x16x64_bf16 v[54:57], v[18:21], a[56:63], v1\n"
"v_smfmac_f32_16x16x64_bf16 v[58:61], v[18:21], a[64:71], v1\n"
"v_smfmac_f32_16x16x64_bf16 v[50:53], v[22:25], a[72:79], v1\n"
"v_smfmac_f32_16x16x64_bf16 v[54:57], v[22:25], a[80:87], v1\n"
"v_smfmac_f32_16x16x64_bf16 v[58:61], v[22:25], a[88:95], v1\n"
"v_smfmac_f32_16x16x64_bf16 v[50:53], v[26:29], a[96:103], v1\n"
"v_smfmac_f32_16x16x64_bf16 v[54:57], v[26:29], a[104:111], v1\n"
"v_smfmac_f32_16x16x64_bf16 v[58:61], v[26:29], a[112:119], v1\n"
"v_smfmac_f32_16x16x64_bf16 v[50:53], v[30:33], a[120:127], v1\n"
"v_smfmac_f32_16x16x64_bf16 v[54:57], v[30:33], a[128:135], v1\n"
"v_smfmac_f32_16x16x64_bf16 v[58:61], v[30:33], a[136:143], v1\n"
"s_branch .Lv_mtail_8\n"
"s_branch .Lv_end\n"
".Lv_abort:\n"
"s_mov_b64 exec, -1\n"
"s_add_u32 s28, s20, 0x617000\n"
"s_addc_u32 s29, s21, 0\n"
"v_mov_b32 v10, 0\n"
"v_mov_b32 v11, s25\n"
"v_or_b32 v11, 0x10000, v11\n"
"global_store_dword v10, v11, s[28:29] sc1\n"
".Lv_end:\n"
"s_waitcnt vmcnt(0) lgkmcnt(0)\n"
"s_branch .Lrole_end\n"
".Lrole_b:\n"
"s_mov_b64 s[20:21], %2\n"
"s_and_b32 s23, %3, 0xff\n"
"s_bfe_u32 s24, %3, 0x10008\n"
"s_bfe_u32 s22, %3, 0x2000a\n"
"s_bfe_u32 s68, %3, 0x3000c\n"
"s_mov_b32 s26, 0\n"
"s_mov_b32 s25, s24\n"
"s_mov_b64 exec, -1\n"
"s_mov_b32 s40, 0xffffffff\n"
"s_mov_b32 s41, 0x0\n"
"s_mov_b32 s42, 0xff00ff00\n"
"s_mov_b32 s43, 0xff00ff00\n"
"s_mov_b32 s44, 0x55555555\n"
"s_mov_b32 s45, 0x55555555\n"
"v_mbcnt_lo_u32_b32 v10, -1, 0\n"
"v_mbcnt_hi_u32_b32 v10, -1, v10\n"
"v_lshlrev_b32 v0, 4, v10\n"
"v_mov_b32 v11, 0x4444\n"
"v_mov_b32 v12, 0xeeee\n"
"v_cndmask_b32 v1, v12, v11, s[44:45]\n"
"s_getpc_b64 s[28:29]\n"
".Lb_pc:\n"
"s_and_b32 s28, s28, 0xffffff80\n"
"v_lshlrev_b32 v200, 3, v0\n"
"s_lshl_b32 s27, s22, 13\n"
"v_add_u32 v200, s27, v200\n"
"v_min_u32 v200, .Lb_end-.Lb_pc-4, v200\n"
"global_load_dword v200, v200, s[28:29]\n"
"s_lshr_b32 s27, s68, 0\n"
"s_xor_b32 s27, s27, s24\n"
"s_and_b32 s27, s27, 1\n"
"s_lshl_b32 s27, s27, 13\n"
"s_add_u32 s27, s27, 0x184000\n"
"s_mul_i32 s28, s23, 6554\n"
"s_lshr_b32 s28, s28, 16\n"
"s_lshl_b32 s28, s28, 8\n"
"s_add_u32 s27, s27, s28\n"
"s_add_u32 s98, s20, s27\n"
"s_addc_u32 s99, s21, 0\n"
"s_lshr_b32 s27, s68, 0\n"
"s_xor_b32 s27, s27, s24\n"
"s_xor_b32 s27, s27, 1\n"
"s_and_b32 s27, s27, 1\n"
"s_lshl_b32 s27, s27, 13\n"
"s_add_u32 s27, s27, 0x180000\n"
"s_add_u32 s38, s20, s27\n"
"s_addc_u32 s39, s21, 0\n"
"s_or_b32 s27, s23, s22\n"
"s_cmp_eq_u32 s27, 0\n"
"s_cselect_b64 s[46:47], 15, 0\n"
"s_mul_i32 s27, s24, 2621440\n"
"s_lshl_b32 s28, s23, 15\n"
"s_add_u32 s27, s27, s28\n"
"s_lshl_b32 s28, s22, 13\n"
"s_add_u32 s27, s27, s28\n"
"s_add_u32 s27, s27, 0xc01000\n"
"s_add_u32 s30, s20, s27\n"
"s_addc_u32 s31, s21, 0\n"
"s_add_u32 s90, s30, 0x140000\n"
"s_addc_u32 s91, s31, 0\n"
"s_lshr_b32 s27, s68, 2\n"
"s_xor_b32 s27, s27, s24\n"
"s_and_b32 s27, s27, 1\n"
"s_lshl_b32 s27, s27, 13\n"
"s_lshl_b32 s28, s23, 14\n"
"s_add_u32 s27, s27, s28\n"
"s_lshl_b32 s28, s22, 11\n"
"s_add_u32 s27, s27, s28\n"
"s_add_u32 s27, s27, 0x400000\n"
"s_add_u32 s96, s20, s27\n"
"s_addc_u32 s97, s21, 0\n"
"s_add_u32 s92, s96, 0xa0000\n"
"s_addc_u32 s93, s97, 0\n"
"s_lshr_b32 s27, s68, 1\n"
"s_xor_b32 s27, s27, s24\n"
"s_and_b32 s27, s27, 1\n"
"s_lshl_b32 s27, s27, 13\n"
"s_lshl_b32 s28, s22, 14\n"
"s_add_u32 s27, s27, s28\n"
"s_add_u32 s27, s27, 0x201000\n"
"s_add_u32 s34, s20, s27\n"
"s_addc_u32 s35, s21, 0\n"
"s_lshr_b32 s27, s23, 2\n"
"s_cmp_ge_u32 s27, 6\n"
"s_cselect_b32 s28, 10240, 0\n"
"s_lshl_b32 s70, s23, 8\n"
"s_add_u32 s70, s70, s28\n"
"s_lshr_b32 s27, s68, 0\n"
"s_xor_b32 s27, s27, s24\n"
"s_xor_b32 s27, s27, 1\n"
"s_and_b32 s27, s27, 1\n"
"s_lshl_b32 s27, s27, 13\n"
"s_add_u32 s27, s27, s70\n"
"s_lshl_b32 s28, s24, 15\n"
"s_add_u32 s27, s27, s28\n"
"s_add_u32 s36, s20, s27\n"
"s_addc_u32 s37, s21, 0\n"
"s_mul_i32 s27, s24, 81920\n"
"s_mul_i32 s28, s22, 10240\n"
"s_add_u32 s27, s27, s28\n"
"s_add_u32 s27, s27, 0x1401000\n"
"s_add_u32 s56, s20, s27\n"
"s_addc_u32 s57, s21, 0\n"
"s_add_u32 s58, s56, 0xa000\n"
"s_addc_u32 s59, s57, 0\n"
"s_mul_i32 s27, s23, 6554\n"
"s_lshr_b32 s27, s27, 16\n"
"s_lshl_b32 s28, s24, 2\n"
"s_add_u32 s27, s27, s28\n"
"s_lshl_b32 s27, s27, 12\n"
"s_lshl_b32 s28, s22, 10\n"
"s_add_u32 s27, s27, s28\n"
"s_add_u32 s27, s27, 0x600000\n"
"s_add_u32 s60, s20, s27\n"
"s_addc_u32 s61, s21, 0\n"
"s_lshl_b32 s27, s24, 13\n"
"s_add_u32 s27, s27, s28\n"
"s_add_u32 s27, s27, 0x608000\n"
"s_add_u32 s62, s20, s27\n"
"s_addc_u32 s63, s21, 0\n"
"s_lshl_b32 s27, s24, 1\n"
"s_and_b32 s28, s22, 1\n"
"s_add_u32 s27, s27, s28\n"
"s_mul_i32 s27, s27, 20480\n"
"s_lshl_b32 s28, s23, 5\n"
"s_add_u32 s27, s27, s28\n"
"s_mov_b64 s[64:65], %0\n"
"s_add_u32 s64, s64, s27\n"
"s_addc_u32 s65, s65, 0\n"
"s_mul_i32 s27, s24, 40960\n"
"s_lshr_b32 s28, s23, 1\n"
"s_lshl_b32 s28, s28, 11\n"
"s_add_u32 s27, s27, s28\n"
"s_and_b32 s28, s22, 1\n"
"s_lshl_b32 s28, s28, 10\n"
"s_add_u32 s27, s27, s28\n"
"s_and_b32 s28, s23, 1\n"
"s_lshl_b32 s28, s28, 9\n"
"s_add_u32 s27, s27, s28\n"
"s_add_u32 s27, s27, 0x1c00000\n"
"s_add_u32 s66, s20, s27\n"
"s_addc_u32 s67, s21, 0\n"
"s_lshl_b32 s27, s24, 1\n"
"s_and_b32 s28, s22, 1\n"
"s_add_u32 s27, s27, s28\n"
"s_mul_i32 s27, s27, 40\n"
"s_add_u32 s27, s27, s23\n"
"s_lshl_b32 s27, s27, 7\n"
"s_add_u32 s27, s27, 0x60c000\n"
"s_add_u32 s40, s20, s27\n"
"s_addc_u32 s41, s21, 0\n"
"v_and_b32 v11, 15, v10\n"
"v_lshrrev_b32 v12, 4, v10\n"
"v_lshlrev_b32 v13, 4, v11\n"
"v_and_b32 v14, 1, v12\n"
"v_lshl_add_u32 v13, v14, 8, v13\n"
"v_lshrrev_b32 v14, 5, v10\n"
"v_lshl_add_u32 v2, v14, 10, v13\n"
"v_lshlrev_b32 v14, 4, v11\n"
"v_lshl_add_u32 v14, v12, 11, v14\n"
"v_add_u32 v3, 512, v14\n"
"v_and_b32 v13, 7, v10\n"
"v_lshlrev_b32 v9, 8, v10\n"
"v_lshlrev_b32 v14, 7, v13\n"
"v_bfe_u32 v15, v10, 3, 1\n"
"v_lshl_add_u32 v4, v15, 12, v14\n"
"v_mov_b32 v192, v14\n"
"v_lshlrev_b32 v14, 7, v12\n"
"v_lshl_add_u32 v14, v11, 2, v14\n"
"s_lshl_b32 s27, s22, 9\n"
"v_add_u32 v5, s27, v14\n"
"v_lshrrev_b32 v14, 3, v10\n"
"v_lshlrev_b32 v14, 6, v14\n"
"v_and_b32 v15, 3, v10\n"
"v_lshl_add_u32 v14, v15, 4, v14\n"
"v_bfe_u32 v15, v10, 2, 1\n"
"v_lshl_add_u32 v6, v15, 3, v14\n"
"v_lshlrev_b32 v7, 2, v10\n"
"v_mul_u32_u24 v14, 1280, v11\n"
"v_lshl_add_u32 v8, v12, 3, v14\n"
"v_and_b32 v14, 15, v10\n"
"v_lshlrev_b32 v14, 4, v14\n"
"v_bfe_u32 v15, v10, 4, 1\n"
"v_lshl_add_u32 v14, v15, 3, v14\n"
"v_lshrrev_b32 v15, 5, v10\n"
"v_lshl_add_u32 v195, v15, 8, v14\n"
"s_lshl_b32 s27, s22, 11\n"
"s_add_u32 s27, s27, 4096\n"
"v_add_u32 v193, s27, v0\n"
"s_and_b32 s27, s22, 1\n"
"s_lshl_b32 s27, s27, 10\n"
"s_add_u32 s27, s27, 4096\n"
"v_add_u32 v194, s27, v0\n"
"v_mov_b32 v188, -1\n"
"v_mov_b32 v189, -1\n"
"v_mov_b32 v190, -1\n"
"v_mov_b32 v191, -1\n"
"global_store_dwordx4 v0, v[188:191], s[92:93] offset:0 sc1\n"
"global_store_dwordx4 v0, v[188:191], s[92:93] offset:1024 sc1\n"
".Lb_top_1:\n"
"s_nop 7\n"
"s_add_u32 s70, s25, 1\n"
"global_load_dwordx2 v[184:185], v8, s[64:65]\n"
".Lb_fw_2:\n"
"global_load_dword v12, v192, s[60:61] sc1\n"
"s_waitcnt vmcnt(0)\n"
"v_cmp_gt_u32 vcc, s70, v12\n"
"s_cbranch_vccz .Lb_fok_3\n"
"s_sleep 2\n"
"s_add_u32 s26, s26, 1\n"
"s_cmp_lt_u32 s26, 0x80000\n"
"s_cbranch_scc1 .Lb_fw_2\n"
"s_branch .Lb_abort\n"
".Lb_fok_3:\n"
"global_load_dwordx4 v[16:19], v0, s[30:31] offset:-4096 sc1\n"
"global_load_dwordx4 v[20:23], v0, s[30:31] offset:-3072 sc1\n"
"global_load_dwordx4 v[24:27], v0, s[30:31] offset:-2048 sc1\n"
"global_load_dwordx4 v[28:31], v0, s[30:31] offset:-1024 sc1\n"
"global_load_dwordx4 v[32:35], v0, s[30:31] offset:0 sc1\n"
"global_load_dwordx4 v[36:39], v0, s[30:31] offset:1024 sc1\n"
"global_load_dwordx4 v[40:43], v0, s[30:31] offset:2048 sc1\n"
"global_load_dwordx4 v[44:47], v0, s[30:31] offset:3072 sc1\n"
"s_add_u32 s27, s25, 2\n"
"v_mov_b32 v14, s27\n"
"s_add_u32 s70, s25, 1\n"
"s_cmp_lg_u32 s22, 0\n"
"s_cbranch_scc1 .Lb_tkok_5\n"
"v_mov_b32 v95, 0\n"
"global_load_dword v92, v95, s[98:99] sc1\n"
".Lb_tk_4:\n"
"global_load_dword v93, v95, s[98:99] sc1\n"
"s_waitcnt vmcnt(1)\n"
"v_cmp_gt_u32 vcc, s70, v92\n"
"s_cbranch_vccz .Lb_tkok_5\n"
"global_load_dword v92, v95, s[98:99] sc1\n"
"s_waitcnt vmcnt(1)\n"
"v_cmp_gt_u32 vcc, s70, v93\n"
"s_cbranch_vccz .Lb_tkok_5\n"
"s_add_u32 s26, s26, 1\n"
"s_cmp_lt_u32 s26, 0x80000\n"
"s_cbranch_scc1 .Lb_tk_4\n"
"s_branch .Lb_abort\n"
".Lb_tkok_5:\n"
"s_barrier\n"
".Lb_pp_6:\n"
"global_load_dwordx4 v[48:51], v0, s[96:97] offset:0 sc1\n"
"global_load_dwordx4 v[52:55], v0, s[96:97] offset:1024 sc1\n"
"global_load_dwordx4 v[72:75], v3, s[34:35] offset:-4096 sc1\n"
"global_load_dwordx4 v[56:59], v2, s[34:35] offset:-4096 sc1\n"
"global_load_dwordx4 v[60:63], v2, s[34:35] offset:-2048 sc1\n"
"global_load_dwordx4 v[64:67], v2, s[34:35] offset:0 sc1\n"
"global_load_dwordx4 v[68:71], v2, s[34:35] offset:2048 sc1\n"
"v_mov_b64 v[80:81], 0\n"
"v_mov_b64 v[82:83], 0\n"
".Lb_ww_8:\n"
"v_mov_b32 v13, v13\n"
"s_getreg_b32 s27, hwreg(HW_REG_IB_STS, 0, 4)\n"
"s_cmp_gt_u32 s27, 4\n"
"s_cbranch_scc1 .Lb_ww_8\n"
"s_waitcnt vmcnt(4)\n"
"v_smfmac_f32_16x16x64_bf16 v[80:83], v[72:75], v[48:55], v1\n"
"v_max3_u32 v12, v48, v50, v52\n"
"v_max3_u32 v13, v54, v72, v73\n"
"v_max3_u32 v12, v12, v74, v75\n"
"s_waitcnt vmcnt(3)\n"
"v_smfmac_f32_16x16x64_bf16 v[80:83], v[56:59], v[16:23], v1\n"
"v_max3_u32 v13, v13, v56, v58\n"
"s_waitcnt vmcnt(2)\n"
"v_smfmac_f32_16x16x64_bf16 v[80:83], v[60:63], v[24:31], v1\n"
"v_max3_u32 v12, v12, v60, v62\n"
"s_waitcnt vmcnt(1)\n"
"v_smfmac_f32_16x16x64_bf16 v[80:83], v[64:67], v[32:39], v1\n"
"v_max3_u32 v13, v13, v64, v66\n"
"s_waitcnt vmcnt(0)\n"
"v_smfmac_f32_16x16x64_bf16 v[80:83], v[68:71], v[40:47], v1\n"
"v_max3_u32 v12, v12, v68, v70\n"
"v_max_u32 v12, v12, v13\n"
"v_cmp_eq_u32 vcc, -1, v12\n"
"s_cbranch_vccz .Lb_pok_7\n"
"s_add_u32 s26, s26, 1\n"
"s_cmp_lt_u32 s26, 0x80000\n"
"s_cbranch_scc1 .Lb_pp_6\n"
"s_branch .Lb_abort\n"
".Lb_pok_7:\n"
"s_nop 3\n"
"v_add_f32 v80, v80, v81\n"
"v_add_f32 v82, v82, v83\n"
"ds_write2_b32 v5, v80, v82 offset1:16\n"
"s_waitcnt lgkmcnt(0)\n"
"s_barrier\n"
"s_cmp_lg_u32 s22, 0\n"
"s_cbranch_scc1 .Lb_nw0_9\n"
"ds_read_b64 v[80:81], v6 offset:0\n"
"ds_read_b64 v[82:83], v6 offset:512\n"
"ds_read_b64 v[84:85], v6 offset:1024\n"
"ds_read_b64 v[86:87], v6 offset:1536\n"
"s_waitcnt lgkmcnt(2)\n"
"v_pk_add_f32 v[80:81], v[80:81], v[82:83]\n"
"s_waitcnt lgkmcnt(0)\n"
"v_pk_add_f32 v[84:85], v[84:85], v[86:87]\n"
"v_pk_add_f32 v[80:81], v[80:81], v[84:85]\n"
"v_cvt_pk_bf16_f32 v80, v80, v81\n"
"s_nop 1\n"
"v_mov_b32_dpp v81, v80 quad_perm:[1,1,3,3] row_mask:0xf bank_mask:0xf\n"
"s_mov_b64 exec, s[44:45]\n"
"global_store_dwordx2 v7, v[80:81], s[36:37] sc1\n"
"s_mov_b64 exec, -1\n"
"s_mov_b64 exec, s[46:47]\n"
"s_cbranch_execz .Lb_nt_10\n"
"global_store_dword v9, v14, s[38:39] sc1\n"
".Lb_nt_10:\n"
"s_mov_b64 exec, -1\n"
".Lb_nw0_9:\n"
"s_barrier\n"
"global_store_dwordx4 v0, v[188:191], s[96:97] offset:0 sc1\n"
"global_store_dwordx4 v0, v[188:191], s[96:97] offset:1024 sc1\n"
"s_cmp_lg_u32 s22, 3\n"
"s_cbranch_scc1 .Lb_ncl_11\n"
"s_cmp_ge_u32 s25, 59\n"
"s_cbranch_scc1 .Lb_ncl_11\n"
"s_add_u32 s28, s36, 0x10000\n"
"s_addc_u32 s29, s37, 0\n"
"global_store_dword v7, v188, s[28:29] sc1\n"
".Lb_ncl_11:\n"
".Lb_fw_12:\n"
"global_load_dword v12, v4, s[62:63] sc1\n"
"s_waitcnt vmcnt(0)\n"
"v_cmp_gt_u32 vcc, s70, v12\n"
"s_cbranch_vccz .Lb_fok_13\n"
"s_sleep 2\n"
"s_add_u32 s26, s26, 1\n"
"s_cmp_lt_u32 s26, 0x80000\n"
"s_cbranch_scc1 .Lb_fw_12\n"
"s_branch .Lb_abort\n"
".Lb_fok_13:\n"
"global_load_dwordx4 v[96:99], v0, s[56:57] offset:-4096 sc1\n"
"global_load_dwordx4 v[100:103], v0, s[56:57] offset:-3072 sc1\n"
"global_load_dwordx4 v[104:107], v0, s[56:57] offset:-2048 sc1\n"
"global_load_dwordx4 v[108:111], v0, s[56:57] offset:-1024 sc1\n"
"global_load_dwordx4 v[112:115], v0, s[56:57] offset:0 sc1\n"
"global_load_dwordx4 v[116:119], v0, s[56:57] offset:1024 sc1\n"
"global_load_dwordx4 v[120:123], v0, s[56:57] offset:2048 sc1\n"
"global_load_dwordx4 v[124:127], v0, s[56:57] offset:3072 sc1\n"
"s_add_u32 s28, s56, 0x1000\n"
"s_addc_u32 s29, s57, 0\n"
"global_load_dwordx4 v[128:131], v0, s[28:29] offset:0 sc1\n"
"global_load_dwordx4 v[132:135], v0, s[28:29] offset:1024 sc1\n"
"global_load_dwordx4 v[136:139], v0, s[58:59] offset:-4096 sc1\n"
"global_load_dwordx4 v[140:143], v0, s[58:59] offset:-3072 sc1\n"
"global_load_dwordx4 v[144:147], v0, s[58:59] offset:-2048 sc1\n"
"global_load_dwordx4 v[148:151], v0, s[58:59] offset:-1024 sc1\n"
"global_load_dwordx4 v[152:155], v0, s[58:59] offset:0 sc1\n"
"global_load_dwordx4 v[156:159], v0, s[58:59] offset:1024 sc1\n"
"global_load_dwordx4 v[160:163], v0, s[58:59] offset:2048 sc1\n"
"global_load_dwordx4 v[164:167], v0, s[58:59] offset:3072 sc1\n"
"s_add_u32 s28, s58, 0x1000\n"
"s_addc_u32 s29, s59, 0\n"
"global_load_dwordx4 v[168:171], v0, s[28:29] offset:0 sc1\n"
"global_load_dwordx4 v[172:175], v0, s[28:29] offset:1024 sc1\n"
"s_waitcnt vmcnt(19)\n"
"v_mfma_f32_16x16x32_bf16 v[176:179], v[16:19], v[96:99], 0\n"
"s_waitcnt vmcnt(18)\n"
"v_mfma_f32_16x16x32_bf16 v[176:179], v[20:23], v[100:103], v[176:179]\n"
"s_waitcnt vmcnt(17)\n"
"v_mfma_f32_16x16x32_bf16 v[176:179], v[24:27], v[104:107], v[176:179]\n"
"s_waitcnt vmcnt(16)\n"
"v_mfma_f32_16x16x32_bf16 v[176:179], v[28:31], v[108:111], v[176:179]\n"
"s_waitcnt vmcnt(15)\n"
"v_mfma_f32_16x16x32_bf16 v[176:179], v[32:35], v[112:115], v[176:179]\n"
"s_waitcnt vmcnt(14)\n"
"v_mfma_f32_16x16x32_bf16 v[176:179], v[36:39], v[116:119], v[176:179]\n"
"s_waitcnt vmcnt(13)\n"
"v_mfma_f32_16x16x32_bf16 v[176:179], v[40:43], v[120:123], v[176:179]\n"
"s_waitcnt vmcnt(12)\n"
"v_mfma_f32_16x16x32_bf16 v[176:179], v[44:47], v[124:127], v[176:179]\n"
"s_waitcnt vmcnt(11)\n"
"v_mfma_f32_16x16x32_bf16 v[176:179], v[48:51], v[128:131], v[176:179]\n"
"s_waitcnt vmcnt(10)\n"
"v_mfma_f32_16x16x32_bf16 v[176:179], v[52:55], v[132:135], v[176:179]\n"
"s_waitcnt vmcnt(9)\n"
"v_mfma_f32_16x16x32_bf16 v[180:183], v[16:19], v[136:139], 0\n"
"s_waitcnt vmcnt(8)\n"
"v_mfma_f32_16x16x32_bf16 v[180:183], v[20:23], v[140:143], v[180:183]\n"
"s_waitcnt vmcnt(7)\n"
"v_mfma_f32_16x16x32_bf16 v[180:183], v[24:27], v[144:147], v[180:183]\n"
"s_waitcnt vmcnt(6)\n"
"v_mfma_f32_16x16x32_bf16 v[180:183], v[28:31], v[148:151], v[180:183]\n"
"s_waitcnt vmcnt(5)\n"
"v_mfma_f32_16x16x32_bf16 v[180:183], v[32:35], v[152:155], v[180:183]\n"
"s_waitcnt vmcnt(4)\n"
"v_mfma_f32_16x16x32_bf16 v[180:183], v[36:39], v[156:159], v[180:183]\n"
"s_waitcnt vmcnt(3)\n"
"v_mfma_f32_16x16x32_bf16 v[180:183], v[40:43], v[160:163], v[180:183]\n"
"s_waitcnt vmcnt(2)\n"
"v_mfma_f32_16x16x32_bf16 v[180:183], v[44:47], v[164:167], v[180:183]\n"
"s_waitcnt vmcnt(1)\n"
"v_mfma_f32_16x16x32_bf16 v[180:183], v[48:51], v[168:171], v[180:183]\n"
"s_waitcnt vmcnt(0)\n"
"v_mfma_f32_16x16x32_bf16 v[180:183], v[52:55], v[172:175], v[180:183]\n"
"ds_write_b128 v193, v[176:179] offset:0\n"
"s_nop 6\n"
"ds_write_b128 v193, v[180:183] offset:1024\n"
"s_waitcnt lgkmcnt(0)\n"
"s_barrier\n"
"s_cmp_ge_u32 s22, 2\n"
"s_cbranch_scc1 .Lb_nw_14\n"
"ds_read_b128 v[96:99], v194 offset:0\n"
"ds_read_b128 v[100:103], v194 offset:2048\n"
"ds_read_b128 v[104:107], v194 offset:4096\n"
"ds_read_b128 v[108:111], v194 offset:6144\n"
"v_lshlrev_b32 v112, 16, v184\n"
"v_and_b32 v113, 0xffff0000, v184\n"
"v_lshlrev_b32 v114, 16, v185\n"
"v_and_b32 v115, 0xffff0000, v185\n"
"s_waitcnt lgkmcnt(3)\n"
"v_add_f32 v112, v112, v96\n"
"v_add_f32 v113, v113, v97\n"
"v_add_f32 v114, v114, v98\n"
"v_add_f32 v115, v115, v99\n"
"s_waitcnt lgkmcnt(2)\n"
"v_add_f32 v112, v112, v100\n"
"v_add_f32 v113, v113, v101\n"
"v_add_f32 v114, v114, v102\n"
"v_add_f32 v115, v115, v103\n"
"s_waitcnt lgkmcnt(1)\n"
"v_add_f32 v112, v112, v104\n"
"v_add_f32 v113, v113, v105\n"
"v_add_f32 v114, v114, v106\n"
"v_add_f32 v115, v115, v107\n"
"s_waitcnt lgkmcnt(0)\n"
"v_add_f32 v112, v112, v108\n"
"v_add_f32 v113, v113, v109\n"
"v_add_f32 v114, v114, v110\n"
"v_add_f32 v115, v115, v111\n"
"v_cvt_pk_bf16_f32 v112, v112, v113\n"
"v_cvt_pk_bf16_f32 v113, v114, v115\n"
"global_store_dwordx2 v195, v[112:113], s[66:67] sc1\n"
"s_waitcnt vmcnt(0)\n"
"s_mov_b64 exec, 1\n"
"s_add_u32 s27, s25, 1\n"
"v_mov_b32 v114, s27\n"
"v_mov_b32 v115, 0\n"
"global_store_dword v115, v114, s[40:41] sc1\n"
"s_mov_b64 exec, -1\n"
".Lb_nw_14:\n"
"s_mov_b64 s[28:29], s[30:31]\n"
"s_mov_b64 s[30:31], s[90:91]\n"
"s_mov_b64 s[90:91], s[28:29]\n"
"s_mov_b64 s[28:29], s[96:97]\n"
"s_mov_b64 s[96:97], s[92:93]\n"
"s_mov_b64 s[92:93], s[28:29]\n"
"s_add_u32 s34, s34, 0x10000\n"
"s_addc_u32 s35, s35, 0\n"
"s_add_u32 s36, s36, 0x8000\n"
"s_addc_u32 s37, s37, 0\n"
"s_add_u32 s56, s56, 0x28000\n"
"s_addc_u32 s57, s57, 0\n"
"s_add_u32 s58, s58, 0x28000\n"
"s_addc_u32 s59, s59, 0\n"
"s_add_u32 s64, s64, 0x14000\n"
"s_addc_u32 s65, s65, 0\n"
"s_add_u32 s66, s66, 0x14000\n"
"s_addc_u32 s67, s67, 0\n"
"s_add_u32 s25, s25, 2\n"
"s_cmp_lt_u32 s25, 64\n"
"s_cbranch_scc1 .Lb_top_1\n"
"s_branch .Lb_end\n"
".Lb_abort:\n"
"s_mov_b64 exec, -1\n"
"s_add_u32 s28, s20, 0x617000\n"
"s_addc_u32 s29, s21, 0\n"
"v_mov_b32 v10, 0\n"
"v_mov_b32 v11, s25\n"
"v_or_b32 v11, 0x10000, v11\n"
"global_store_dword v10, v11, s[28:29] sc1\n"
".Lb_end:\n"
"s_waitcnt vmcnt(0) lgkmcnt(0)\n"
"s_branch .Lrole_end\n"
".Lrole_f:\n"
"s_mov_b64 s[20:21], %2\n"
"s_and_b32 s23, %3, 0xff\n"
"s_bfe_u32 s24, %3, 0x10008\n"
"s_bfe_u32 s22, %3, 0x2000a\n"
"s_bfe_u32 s68, %3, 0x3000c\n"
"s_mov_b32 s26, 0\n"
"s_mov_b32 s25, s24\n"
"s_mov_b64 exec, -1\n"
"s_mov_b32 s40, 0xffffffff\n"
"s_mov_b32 s41, 0x0\n"
"s_mov_b32 s42, 0xff00ff00\n"
"s_mov_b32 s43, 0xff00ff00\n"
"s_mov_b32 s44, 0x55555555\n"
"s_mov_b32 s45, 0x55555555\n"
"v_mbcnt_lo_u32_b32 v10, -1, 0\n"
"v_mbcnt_hi_u32_b32 v10, -1, v10\n"
"v_lshlrev_b32 v0, 4, v10\n"
"v_mov_b32 v11, 0x4444\n"
"v_mov_b32 v12, 0xeeee\n"
"v_cndmask_b32 v1, v12, v11, s[44:45]\n"
"s_getpc_b64 s[28:29]\n"
".Lf_pc:\n"
"s_and_b32 s28, s28, 0xffffff80\n"
"v_lshlrev_b32 v200, 3, v0\n"
"s_lshl_b32 s27, s22, 13\n"
"v_add_u32 v200, s27, v200\n"
"v_min_u32 v200, .Lf_end-.Lf_pc-4, v200\n"
"global_load_dword v200, v200, s[28:29]\n"
"s_sleep 127\n"
"s_sleep 127\n"
"s_lshl_b32 s70, s23, 2\n"
"s_add_u32 s70, s70, s22\n"
"s_lshl_b32 s27, s70, 6\n"
"s_mov_b64 s[30:31], %0\n"
"s_add_u32 s30, s30, s27\n"
"s_addc_u32 s31, s31, 0\n"
"s_mov_b64 s[34:35], %1\n"
"s_add_u32 s34, s34, s27\n"
"s_addc_u32 s35, s35, 0\n"
"v_lshrrev_b32 v11, 4, v10\n"
"v_lshlrev_b32 v11, 3, v11\n"
"global_load_dwordx2 v[32:33], v11, s[34:35] offset:0\n"
"global_load_dwordx2 v[34:35], v11, s[34:35] offset:32\n"
"s_waitcnt vmcnt(1)\n"
"v_lshlrev_b32 v16, 16, v32\n"
"v_and_b32 v17, 0xffff0000, v32\n"
"v_lshlrev_b32 v18, 16, v33\n"
"v_and_b32 v19, 0xffff0000, v33\n"
"s_waitcnt vmcnt(0)\n"
"v_lshlrev_b32 v20, 16, v34\n"
"v_and_b32 v21, 0xffff0000, v34\n"
"v_lshlrev_b32 v22, 16, v35\n"
"v_and_b32 v23, 0xffff0000, v35\n"
"s_mul_i32 s27, s24, 98304\n"
"s_lshl_b32 s28, s70, 11\n"
"s_add_u32 s27, s27, s28\n"
"s_add_u32 s27, s27, 0x2000000\n"
"s_add_u32 s56, s20, s27\n"
"s_addc_u32 s57, s21, 0\n"
"s_mul_i32 s27, s24, 48\n"
"s_add_u32 s27, s27, s70\n"
"s_lshl_b32 s27, s27, 7\n"
"s_add_u32 s27, s27, 0x612000\n"
"s_add_u32 s58, s20, s27\n"
"s_addc_u32 s59, s21, 0\n"
"s_mul_i32 s27, s24, 40960\n"
"s_mul_i32 s28, s22, 10240\n"
"s_add_u32 s27, s27, s28\n"
"s_add_u32 s27, s27, 0x1c01000\n"
"s_add_u32 s34, s20, s27\n"
"s_addc_u32 s35, s21, 0\n"
"s_mul_i32 s27, s24, 10240\n"
"s_mul_i32 s28, s22, 1280\n"
"s_add_u32 s27, s27, s28\n"
"s_add_u32 s27, s27, 0x60c000\n"
"s_add_u32 s38, s20, s27\n"
"s_addc_u32 s39, s21, 0\n"
"s_mov_b32 s60, 0xf0000f\n"
"s_mov_b32 s61, 0xf0000f00\n"
"s_mov_b32 s48, 0x22222222\n"
"s_mov_b32 s49, 0x22222222\n"
"s_mov_b32 s50, 0x44444444\n"
"s_mov_b32 s51, 0x44444444\n"
"s_mov_b32 s52, 0x88888888\n"
"s_mov_b32 s53, 0x88888888\n"
"v_min_u32 v11, 19, v10\n"
"v_cmp_lt_u32 vcc, 9, v11\n"
"v_add_u32 v12, 30, v11\n"
"s_nop 0\n"
"v_cndmask_b32 v11, v11, v12, vcc\n"
"v_lshlrev_b32 v2, 7, v11\n"
"s_mul_i32 s27, s22, 10240\n"
"v_add_u32 v3, s27, v0\n"
"v_and_b32 v11, 15, v10\n"
"v_lshlrev_b32 v11, 5, v11\n"
"v_add_u32 v5, 40960, v11\n"
"s_lshl_b32 s27, s22, 3\n"
"v_add_u32 v4, s27, v5\n"
"v_lshrrev_b32 v11, 4, v10\n"
"v_lshlrev_b32 v11, 3, v11\n"
"v_bfe_u32 v12, v10, 2, 2\n"
"v_add_u32 v11, v11, v12\n"
"v_lshlrev_b32 v11, 6, v11\n"
"v_and_b32 v12, 3, v10\n"
"v_lshl_add_u32 v11, v12, 3, v11\n"
"s_mul_i32 s27, s22, 20480\n"
"v_add_u32 v6, s27, v11\n"
"v_lshrrev_b32 v11, 2, v10\n"
"v_mul_u32_u24 v11, 3072, v11\n"
"v_and_b32 v12, 3, v10\n"
"v_lshl_add_u32 v7, v12, 4, v11\n"
"v_mov_b32 v12, 0x3f803f80\n"
"v_mov_b32 v13, 0x3f803f80\n"
"v_mov_b32 v14, 0x3f803f80\n"
"v_mov_b32 v15, 0x3f803f80\n"
"s_mul_i32 s27, s22, 20480\n"
"s_mov_b32 m0, s27\n"
"s_add_u32 s27, s27, 1024\n"
"global_load_lds_dwordx4 v7, s[30:31]\n"
"s_add_u32 s30, s30, 0xc000\n"
"s_addc_u32 s31, s31, 0\n"
"s_mov_b32 m0, s27\n"
"s_add_u32 s27, s27, 1024\n"
"global_load_lds_dwordx4 v7, s[30:31]\n"
"s_add_u32 s30, s30, 0xc000\n"
"s_addc_u32 s31, s31, 0\n"
"s_mov_b32 m0, s27\n"
"s_add_u32 s27, s27, 1024\n"
"global_load_lds_dwordx4 v7, s[30:31]\n"
"s_add_u32 s30, s30, 0xc000\n"
"s_addc_u32 s31, s31, 0\n"
"s_mov_b32 m0, s27\n"
"s_add_u32 s27, s27, 1024\n"
"global_load_lds_dwordx4 v7, s[30:31]\n"
"s_add_u32 s30, s30, 0xc000\n"
"s_addc_u32 s31, s31, 0\n"
"s_mov_b32 m0, s27\n"
"s_add_u32 s27, s27, 1024\n"
"global_load_lds_dwordx4 v7, s[30:31]\n"
"s_add_u32 s30, s30, 0xc000\n"
"s_addc_u32 s31, s31, 0\n"
"s_mov_b32 m0, s27\n"
"s_add_u32 s27, s27, 1024\n"
"global_load_lds_dwordx4 v7, s[30:31]\n"
"s_add_u32 s30, s30, 0xc000\n"
"s_addc_u32 s31, s31, 0\n"
"s_mov_b32 m0, s27\n"
"s_add_u32 s27, s27, 1024\n"
"global_load_lds_dwordx4 v7, s[30:31]\n"
"s_add_u32 s30, s30, 0xc000\n"
"s_addc_u32 s31, s31, 0\n"
"s_mov_b32 m0, s27\n"
"s_add_u32 s27, s27, 1024\n"
"global_load_lds_dwordx4 v7, s[30:31]\n"
"s_add_u32 s30, s30, 0xc000\n"
"s_addc_u32 s31, s31, 0\n"
"s_mov_b32 m0, s27\n"
"s_add_u32 s27, s27, 1024\n"
"global_load_lds_dwordx4 v7, s[30:31]\n"
"s_add_u32 s30, s30, 0xc000\n"
"s_addc_u32 s31, s31, 0\n"
"s_mov_b32 m0, s27\n"
"s_add_u32 s27, s27, 1024\n"
"global_load_lds_dwordx4 v7, s[30:31]\n"
"s_add_u32 s30, s30, 0xc000\n"
"s_addc_u32 s31, s31, 0\n"
"s_mov_b32 m0, s27\n"
"s_add_u32 s27, s27, 1024\n"
"global_load_lds_dwordx4 v7, s[30:31]\n"
"s_add_u32 s30, s30, 0xc000\n"
"s_addc_u32 s31, s31, 0\n"
"s_mov_b32 m0, s27\n"
"s_add_u32 s27, s27, 1024\n"
"global_load_lds_dwordx4 v7, s[30:31]\n"
"s_add_u32 s30, s30, 0xc000\n"
"s_addc_u32 s31, s31, 0\n"
"s_mov_b32 m0, s27\n"
"s_add_u32 s27, s27, 1024\n"
"global_load_lds_dwordx4 v7, s[30:31]\n"
"s_add_u32 s30, s30, 0xc000\n"
"s_addc_u32 s31, s31, 0\n"
"s_mov_b32 m0, s27\n"
"s_add_u32 s27, s27, 1024\n"
"global_load_lds_dwordx4 v7, s[30:31]\n"
"s_add_u32 s30, s30, 0xc000\n"
"s_addc_u32 s31, s31, 0\n"
"s_mov_b32 m0, s27\n"
"s_add_u32 s27, s27, 1024\n"
"global_load_lds_dwordx4 v7, s[30:31]\n"
"s_add_u32 s30, s30, 0xc000\n"
"s_addc_u32 s31, s31, 0\n"
"s_mov_b32 m0, s27\n"
"s_add_u32 s27, s27, 1024\n"
"global_load_lds_dwordx4 v7, s[30:31]\n"
"s_add_u32 s30, s30, 0xc000\n"
"s_addc_u32 s31, s31, 0\n"
"s_mov_b32 m0, s27\n"
"s_add_u32 s27, s27, 1024\n"
"global_load_lds_dwordx4 v7, s[30:31]\n"
"s_add_u32 s30, s30, 0xc000\n"
"s_addc_u32 s31, s31, 0\n"
"s_mov_b32 m0, s27\n"
"s_add_u32 s27, s27, 1024\n"
"global_load_lds_dwordx4 v7, s[30:31]\n"
"s_add_u32 s30, s30, 0xc000\n"
"s_addc_u32 s31, s31, 0\n"
"s_mov_b32 m0, s27\n"
"s_add_u32 s27, s27, 1024\n"
"global_load_lds_dwordx4 v7, s[30:31]\n"
"s_add_u32 s30, s30, 0xc000\n"
"s_addc_u32 s31, s31, 0\n"
"s_mov_b32 m0, s27\n"
"s_add_u32 s27, s27, 1024\n"
"global_load_lds_dwordx4 v7, s[30:31]\n"
"s_add_u32 s30, s30, 0xc000\n"
"s_addc_u32 s31, s31, 0\n"
"s_waitcnt vmcnt(0)\n"
"ds_read_b64_tr_b16 a[0:1], v6 offset:0\n"
"ds_read_b64_tr_b16 a[2:3], v6 offset:256\n"
"ds_read_b64_tr_b16 a[80:81], v6 offset:32\n"
"ds_read_b64_tr_b16 a[82:83], v6 offset:288\n"
"ds_read_b64_tr_b16 a[4:5], v6 offset:2048\n"
"ds_read_b64_tr_b16 a[6:7], v6 offset:2304\n"
"ds_read_b64_tr_b16 a[84:85], v6 offset:2080\n"
"ds_read_b64_tr_b16 a[86:87], v6 offset:2336\n"
"ds_read_b64_tr_b16 a[8:9], v6 offset:4096\n"
"ds_read_b64_tr_b16 a[10:11], v6 offset:4352\n"
"ds_read_b64_tr_b16 a[88:89], v6 offset:4128\n"
"ds_read_b64_tr_b16 a[90:91], v6 offset:4384\n"
"ds_read_b64_tr_b16 a[12:13], v6 offset:6144\n"
"ds_read_b64_tr_b16 a[14:15], v6 offset:6400\n"
"ds_read_b64_tr_b16 a[92:93], v6 offset:6176\n"
"ds_read_b64_tr_b16 a[94:95], v6 offset:6432\n"
"ds_read_b64_tr_b16 a[16:17], v6 offset:8192\n"
"ds_read_b64_tr_b16 a[18:19], v6 offset:8448\n"
"ds_read_b64_tr_b16 a[96:97], v6 offset:8224\n"
"ds_read_b64_tr_b16 a[98:99], v6 offset:8480\n"
"ds_read_b64_tr_b16 a[20:21], v6 offset:10240\n"
"ds_read_b64_tr_b16 a[22:23], v6 offset:10496\n"
"ds_read_b64_tr_b16 a[100:101], v6 offset:10272\n"
"ds_read_b64_tr_b16 a[102:103], v6 offset:10528\n"
"ds_read_b64_tr_b16 a[24:25], v6 offset:12288\n"
"ds_read_b64_tr_b16 a[26:27], v6 offset:12544\n"
"ds_read_b64_tr_b16 a[104:105], v6 offset:12320\n"
"ds_read_b64_tr_b16 a[106:107], v6 offset:12576\n"
"ds_read_b64_tr_b16 a[28:29], v6 offset:14336\n"
"ds_read_b64_tr_b16 a[30:31], v6 offset:14592\n"
"ds_read_b64_tr_b16 a[108:109], v6 offset:14368\n"
"ds_read_b64_tr_b16 a[110:111], v6 offset:14624\n"
"ds_read_b64_tr_b16 a[32:33], v6 offset:16384\n"
"ds_read_b64_tr_b16 a[34:35], v6 offset:16640\n"
"ds_read_b64_tr_b16 a[112:113], v6 offset:16416\n"
"ds_read_b64_tr_b16 a[114:115], v6 offset:16672\n"
"ds_read_b64_tr_b16 a[36:37], v6 offset:18432\n"
"ds_read_b64_tr_b16 a[38:39], v6 offset:18688\n"
"ds_read_b64_tr_b16 a[116:117], v6 offset:18464\n"
"ds_read_b64_tr_b16 a[118:119], v6 offset:18720\n"
"s_waitcnt lgkmcnt(0)\n"
"s_mul_i32 s27, s22, 20480\n"
"s_mov_b32 m0, s27\n"
"s_add_u32 s27, s27, 1024\n"
"global_load_lds_dwordx4 v7, s[30:31]\n"
"s_add_u32 s30, s30, 0xc000\n"
"s_addc_u32 s31, s31, 0\n"
"s_mov_b32 m0, s27\n"
"s_add_u32 s27, s27, 1024\n"
"global_load_lds_dwordx4 v7, s[30:31]\n"
"s_add_u32 s30, s30, 0xc000\n"
"s_addc_u32 s31, s31, 0\n"
"s_mov_b32 m0, s27\n"
"s_add_u32 s27, s27, 1024\n"
"global_load_lds_dwordx4 v7, s[30:31]\n"
"s_add_u32 s30, s30, 0xc000\n"
"s_addc_u32 s31, s31, 0\n"
"s_mov_b32 m0, s27\n"
"s_add_u32 s27, s27, 1024\n"
"global_load_lds_dwordx4 v7, s[30:31]\n"
"s_add_u32 s30, s30, 0xc000\n"
"s_addc_u32 s31, s31, 0\n"
"s_mov_b32 m0, s27\n"
"s_add_u32 s27, s27, 1024\n"
"global_load_lds_dwordx4 v7, s[30:31]\n"
"s_add_u32 s30, s30, 0xc000\n"
"s_addc_u32 s31, s31, 0\n"
"s_mov_b32 m0, s27\n"
"s_add_u32 s27, s27, 1024\n"
"global_load_lds_dwordx4 v7, s[30:31]\n"
"s_add_u32 s30, s30, 0xc000\n"
"s_addc_u32 s31, s31, 0\n"
"s_mov_b32 m0, s27\n"
"s_add_u32 s27, s27, 1024\n"
"global_load_lds_dwordx4 v7, s[30:31]\n"
"s_add_u32 s30, s30, 0xc000\n"
"s_addc_u32 s31, s31, 0\n"
"s_mov_b32 m0, s27\n"
"s_add_u32 s27, s27, 1024\n"
"global_load_lds_dwordx4 v7, s[30:31]\n"
"s_add_u32 s30, s30, 0xc000\n"
"s_addc_u32 s31, s31, 0\n"
"s_mov_b32 m0, s27\n"
"s_add_u32 s27, s27, 1024\n"
"global_load_lds_dwordx4 v7, s[30:31]\n"
"s_add_u32 s30, s30, 0xc000\n"
"s_addc_u32 s31, s31, 0\n"
"s_mov_b32 m0, s27\n"
"s_add_u32 s27, s27, 1024\n"
"global_load_lds_dwordx4 v7, s[30:31]\n"
"s_add_u32 s30, s30, 0xc000\n"
"s_addc_u32 s31, s31, 0\n"
"s_mov_b32 m0, s27\n"
"s_add_u32 s27, s27, 1024\n"
"global_load_lds_dwordx4 v7, s[30:31]\n"
"s_add_u32 s30, s30, 0xc000\n"
"s_addc_u32 s31, s31, 0\n"
"s_mov_b32 m0, s27\n"
"s_add_u32 s27, s27, 1024\n"
"global_load_lds_dwordx4 v7, s[30:31]\n"
"s_add_u32 s30, s30, 0xc000\n"
"s_addc_u32 s31, s31, 0\n"
"s_mov_b32 m0, s27\n"
"s_add_u32 s27, s27, 1024\n"
"global_load_lds_dwordx4 v7, s[30:31]\n"
"s_add_u32 s30, s30, 0xc000\n"
"s_addc_u32 s31, s31, 0\n"
"s_mov_b32 m0, s27\n"
"s_add_u32 s27, s27, 1024\n"
"global_load_lds_dwordx4 v7, s[30:31]\n"
"s_add_u32 s30, s30, 0xc000\n"
"s_addc_u32 s31, s31, 0\n"
"s_mov_b32 m0, s27\n"
"s_add_u32 s27, s27, 1024\n"
"global_load_lds_dwordx4 v7, s[30:31]\n"
"s_add_u32 s30, s30, 0xc000\n"
"s_addc_u32 s31, s31, 0\n"
"s_mov_b32 m0, s27\n"
"s_add_u32 s27, s27, 1024\n"
"global_load_lds_dwordx4 v7, s[30:31]\n"
"s_add_u32 s30, s30, 0xc000\n"
"s_addc_u32 s31, s31, 0\n"
"s_mov_b32 m0, s27\n"
"s_add_u32 s27, s27, 1024\n"
"global_load_lds_dwordx4 v7, s[30:31]\n"
"s_add_u32 s30, s30, 0xc000\n"
"s_addc_u32 s31, s31, 0\n"
"s_mov_b32 m0, s27\n"
"s_add_u32 s27, s27, 1024\n"
"global_load_lds_dwordx4 v7, s[30:31]\n"
"s_add_u32 s30, s30, 0xc000\n"
"s_addc_u32 s31, s31, 0\n"
"s_mov_b32 m0, s27\n"
"s_add_u32 s27, s27, 1024\n"
"global_load_lds_dwordx4 v7, s[30:31]\n"
"s_add_u32 s30, s30, 0xc000\n"
"s_addc_u32 s31, s31, 0\n"
"s_mov_b32 m0, s27\n"
"s_add_u32 s27, s27, 1024\n"
"global_load_lds_dwordx4 v7, s[30:31]\n"
"s_add_u32 s30, s30, 0xc000\n"
"s_addc_u32 s31, s31, 0\n"
"s_waitcnt vmcnt(0)\n"
"ds_read_b64_tr_b16 a[40:41], v6 offset:0\n"
"ds_read_b64_tr_b16 a[42:43], v6 offset:256\n"
"ds_read_b64_tr_b16 a[120:121], v6 offset:32\n"
"ds_read_b64_tr_b16 a[122:123], v6 offset:288\n"
"ds_read_b64_tr_b16 a[44:45], v6 offset:2048\n"
"ds_read_b64_tr_b16 a[46:47], v6 offset:2304\n"
"ds_read_b64_tr_b16 a[124:125], v6 offset:2080\n"
"ds_read_b64_tr_b16 a[126:127], v6 offset:2336\n"
"ds_read_b64_tr_b16 a[48:49], v6 offset:4096\n"
"ds_read_b64_tr_b16 a[50:51], v6 offset:4352\n"
"ds_read_b64_tr_b16 a[128:129], v6 offset:4128\n"
"ds_read_b64_tr_b16 a[130:131], v6 offset:4384\n"
"ds_read_b64_tr_b16 a[52:53], v6 offset:6144\n"
"ds_read_b64_tr_b16 a[54:55], v6 offset:6400\n"
"ds_read_b64_tr_b16 a[132:133], v6 offset:6176\n"
"ds_read_b64_tr_b16 a[134:135], v6 offset:6432\n"
"ds_read_b64_tr_b16 a[56:57], v6 offset:8192\n"
"ds_read_b64_tr_b16 a[58:59], v6 offset:8448\n"
"ds_read_b64_tr_b16 a[136:137], v6 offset:8224\n"
"ds_read_b64_tr_b16 a[138:139], v6 offset:8480\n"
"ds_read_b64_tr_b16 a[60:61], v6 offset:10240\n"
"ds_read_b64_tr_b16 a[62:63], v6 offset:10496\n"
"ds_read_b64_tr_b16 a[140:141], v6 offset:10272\n"
"ds_read_b64_tr_b16 a[142:143], v6 offset:10528\n"
"ds_read_b64_tr_b16 a[64:65], v6 offset:12288\n"
"ds_read_b64_tr_b16 a[66:67], v6 offset:12544\n"
"ds_read_b64_tr_b16 a[144:145], v6 offset:12320\n"
"ds_read_b64_tr_b16 a[146:147], v6 offset:12576\n"
"ds_read_b64_tr_b16 a[68:69], v6 offset:14336\n"
"ds_read_b64_tr_b16 a[70:71], v6 offset:14592\n"
"ds_read_b64_tr_b16 a[148:149], v6 offset:14368\n"
"ds_read_b64_tr_b16 a[150:151], v6 offset:14624\n"
"ds_read_b64_tr_b16 a[72:73], v6 offset:16384\n"
"ds_read_b64_tr_b16 a[74:75], v6 offset:16640\n"
"ds_read_b64_tr_b16 a[152:153], v6 offset:16416\n"
"ds_read_b64_tr_b16 a[154:155], v6 offset:16672\n"
"ds_read_b64_tr_b16 a[76:77], v6 offset:18432\n"
"ds_read_b64_tr_b16 a[78:79], v6 offset:18688\n"
"ds_read_b64_tr_b16 a[156:157], v6 offset:18464\n"
"ds_read_b64_tr_b16 a[158:159], v6 offset:18720\n"
"s_waitcnt lgkmcnt(0)\n"
"v_mfma_f32_16x16x32_bf16 v[24:27], a[0:3], v[12:15], 0\n"
"v_mfma_f32_16x16x32_bf16 v[28:31], a[80:83], v[12:15], 0\n"
"v_mfma_f32_16x16x32_bf16 v[24:27], a[4:7], v[12:15], v[24:27]\n"
"v_mfma_f32_16x16x32_bf16 v[28:31], a[84:87], v[12:15], v[28:31]\n"
"v_mfma_f32_16x16x32_bf16 v[24:27], a[8:11], v[12:15], v[24:27]\n"
"v_mfma_f32_16x16x32_bf16 v[28:31], a[88:91], v[12:15], v[28:31]\n"
"v_mfma_f32_16x16x32_bf16 v[24:27], a[12:15], v[12:15], v[24:27]\n"
"v_mfma_f32_16x16x32_bf16 v[28:31], a[92:95], v[12:15], v[28:31]\n"
"v_mfma_f32_16x16x32_bf16 v[24:27], a[16:19], v[12:15], v[24:27]\n"
"v_mfma_f32_16x16x32_bf16 v[28:31], a[96:99], v[12:15], v[28:31]\n"
"v_mfma_f32_16x16x32_bf16 v[24:27], a[20:23], v[12:15], v[24:27]\n"
"v_mfma_f32_16x16x32_bf16 v[28:31], a[100:103], v[12:15], v[28:31]\n"
"v_mfma_f32_16x16x32_bf16 v[24:27], a[24:27], v[12:15], v[24:27]\n"
"v_mfma_f32_16x16x32_bf16 v[28:31], a[104:107], v[12:15], v[28:31]\n"
"v_mfma_f32_16x16x32_bf16 v[24:27], a[28:31], v[12:15], v[24:27]\n"
"v_mfma_f32_16x16x32_bf16 v[28:31], a[108:111], v[12:15], v[28:31]\n"
"v_mfma_f32_16x16x32_bf16 v[24:27], a[32:35], v[12:15], v[24:27]\n"
"v_mfma_f32_16x16x32_bf16 v[28:31], a[112:115], v[12:15], v[28:31]\n"
"v_mfma_f32_16x16x32_bf16 v[24:27], a[36:39], v[12:15], v[24:27]\n"
"v_mfma_f32_16x16x32_bf16 v[28:31], a[116:119], v[12:15], v[28:31]\n"
"v_mfma_f32_16x16x32_bf16 v[24:27], a[40:43], v[12:15], v[24:27]\n"
"v_mfma_f32_16x16x32_bf16 v[28:31], a[120:123], v[12:15], v[28:31]\n"
"v_mfma_f32_16x16x32_bf16 v[24:27], a[44:47], v[12:15], v[24:27]\n"
"v_mfma_f32_16x16x32_bf16 v[28:31], a[124:127], v[12:15], v[28:31]\n"
"v_mfma_f32_16x16x32_bf16 v[24:27], a[48:51], v[12:15], v[24:27]\n"
"v_mfma_f32_16x16x32_bf16 v[28:31], a[128:131], v[12:15], v[28:31]\n"
"v_mfma_f32_16x16x32_bf16 v[24:27], a[52:55], v[12:15], v[24:27]\n"
"v_mfma_f32_16x16x32_bf16 v[28:31], a[132:135], v[12:15], v[28:31]\n"
"v_mfma_f32_16x16x32_bf16 v[24:27], a[56:59], v[12:15], v[24:27]\n"
"v_mfma_f32_16x16x32_bf16 v[28:31], a[136:139], v[12:15], v[28:31]\n"
"v_mfma_f32_16x16x32_bf16 v[24:27], a[60:63], v[12:15], v[24:27]\n"
"v_mfma_f32_16x16x32_bf16 v[28:31], a[140:143], v[12:15], v[28:31]\n"
"v_mfma_f32_16x16x32_bf16 v[24:27], a[64:67], v[12:15], v[24:27]\n"
"v_mfma_f32_16x16x32_bf16 v[28:31], a[144:147], v[12:15], v[28:31]\n"
"v_mfma_f32_16x16x32_bf16 v[24:27], a[68:71], v[12:15], v[24:27]\n"
"v_mfma_f32_16x16x32_bf16 v[28:31], a[148:151], v[12:15], v[28:31]\n"
"v_mfma_f32_16x16x32_bf16 v[24:27], a[72:75], v[12:15], v[24:27]\n"
"v_mfma_f32_16x16x32_bf16 v[28:31], a[152:155], v[12:15], v[28:31]\n"
"v_mfma_f32_16x16x32_bf16 v[24:27], a[76:79], v[12:15], v[24:27]\n"
"v_mfma_f32_16x16x32_bf16 v[28:31], a[156:159], v[12:15], v[28:31]\n"
"s_barrier\n"
"v_mov_b32 v8, 0x3fb8aa3b\n"
"v_mov_b32 v168, 0x40000000\n"
".Lf_top_1:\n"
"s_nop 7\n"
"s_add_u32 s70, s25, 1\n"
".Lf_fw_2:\n"
"global_load_dword v9, v2, s[38:39] sc1\n"
"s_waitcnt vmcnt(0)\n"
"v_cmp_gt_u32 vcc, s70, v9\n"
"s_cbranch_vccz .Lf_fok_3\n"
"s_sleep 10\n"
"s_add_u32 s26, s26, 1\n"
"s_cmp_lt_u32 s26, 0x80000\n"
"s_cbranch_scc1 .Lf_fw_2\n"
"s_branch .Lf_abort\n"
".Lf_fok_3:\n"
"s_add_u32 s36, s34, 0x1000\n"
"s_addc_u32 s37, s35, 0\n"
"global_load_dwordx4 v[32:35], v0, s[34:35] offset:-4096 sc1\n"
"global_load_dwordx4 v[36:39], v0, s[34:35] offset:-3072 sc1\n"
"global_load_dwordx4 v[40:43], v0, s[34:35] offset:-2048 sc1\n"
"global_load_dwordx4 v[44:47], v0, s[34:35] offset:-1024 sc1\n"
"global_load_dwordx4 v[48:51], v0, s[34:35] offset:0 sc1\n"
"global_load_dwordx4 v[52:55], v0, s[34:35] offset:1024 sc1\n"
"global_load_dwordx4 v[56:59], v0, s[34:35] offset:2048 sc1\n"
"global_load_dwordx4 v[60:63], v0, s[34:35] offset:3072 sc1\n"
"global_load_dwordx4 v[64:67], v0, s[36:37] offset:0 sc1\n"
"global_load_dwordx4 v[68:71], v0, s[36:37] offset:1024 sc1\n"
"s_barrier\n"
"s_waitcnt vmcnt(9)\n"
"ds_write_b128 v3, v[32:35] offset:0\n"
"v_mfma_f32_16x16x32_bf16 v[72:75], v[12:15], v[32:35], 0\n"
"v_mfma_f32_16x16x32_bf16 v[80:83], v[32:35], v[32:35], 0\n"
"s_waitcnt vmcnt(8)\n"
"ds_write_b128 v3, v[36:39] offset:1024\n"
"v_mfma_f32_16x16x32_bf16 v[76:79], v[12:15], v[36:39], 0\n"
"v_mfma_f32_16x16x32_bf16 v[84:87], v[36:39], v[36:39], 0\n"
"s_waitcnt vmcnt(7)\n"
"ds_write_b128 v3, v[40:43] offset:2048\n"
"v_mfma_f32_16x16x32_bf16 v[72:75], v[12:15], v[40:43], v[72:75]\n"
"v_mfma_f32_16x16x32_bf16 v[80:83], v[40:43], v[40:43], v[80:83]\n"
"s_waitcnt vmcnt(6)\n"
"ds_write_b128 v3, v[44:47] offset:3072\n"
"v_mfma_f32_16x16x32_bf16 v[76:79], v[12:15], v[44:47], v[76:79]\n"
"v_mfma_f32_16x16x32_bf16 v[84:87], v[44:47], v[44:47], v[84:87]\n"
"s_waitcnt vmcnt(5)\n"
"ds_write_b128 v3, v[48:51] offset:4096\n"
"v_mfma_f32_16x16x32_bf16 v[72:75], v[12:15], v[48:51], v[72:75]\n"
"v_mfma_f32_16x16x32_bf16 v[80:83], v[48:51], v[48:51], v[80:83]\n"
"s_waitcnt vmcnt(4)\n"
"ds_write_b128 v3, v[52:55] offset:5120\n"
"v_mfma_f32_16x16x32_bf16 v[76:79], v[12:15], v[52:55], v[76:79]\n"
"v_mfma_f32_16x16x32_bf16 v[84:87], v[52:55], v[52:55], v[84:87]\n"
"s_waitcnt vmcnt(3)\n"
"ds_write_b128 v3, v[56:59] offset:6144\n"
"v_mfma_f32_16x16x32_bf16 v[72:75], v[12:15], v[56:59], v[72:75]\n"
"v_mfma_f32_16x16x32_bf16 v[80:83], v[56:59], v[56:59], v[80:83]\n"
"s_waitcnt vmcnt(2)\n"
"ds_write_b128 v3, v[60:63] offset:7168\n"
"v_mfma_f32_16x16x32_bf16 v[76:79], v[12:15], v[60:63], v[76:79]\n"
"v_mfma_f32_16x16x32_bf16 v[84:87], v[60:63], v[60:63], v[84:87]\n"
"s_waitcnt vmcnt(1)\n"
"ds_write_b128 v3, v[64:67] offset:8192\n"
"v_mfma_f32_16x16x32_bf16 v[72:75], v[12:15], v[64:67], v[72:75]\n"
"v_mfma_f32_16x16x32_bf16 v[80:83], v[64:67], v[64:67], v[80:83]\n"
"s_waitcnt vmcnt(0)\n"
"ds_write_b128 v3, v[68:71] offset:9216\n"
"v_mfma_f32_16x16x32_bf16 v[76:79], v[12:15], v[68:71], v[76:79]\n"
"v_mfma_f32_16x16x32_bf16 v[84:87], v[68:71], v[68:71], v[84:87]\n"
"s_nop 3\n"
"v_cndmask_b32 v80, v80, v81, s[48:49]\n"
"v_cndmask_b32 v80, v80, v82, s[50:51]\n"
"v_cndmask_b32 v80, v80, v83, s[52:53]\n"
"v_mov_b32 v73, v80\n"
"v_cndmask_b32 v84, v84, v85, s[48:49]\n"
"v_cndmask_b32 v84, v84, v86, s[50:51]\n"
"v_cndmask_b32 v84, v84, v87, s[52:53]\n"
"v_mov_b32 v77, v84\n"
"s_mov_b64 exec, s[60:61]\n"
"ds_write_b64 v4, v[72:73] offset:0\n"
"ds_write_b64 v4, v[76:77] offset:512\n"
"s_mov_b64 exec, -1\n"
"s_waitcnt lgkmcnt(0)\n"
"s_barrier\n"
"ds_read_b128 v[104:107], v0 offset:0\n"
"ds_read_b128 v[108:111], v0 offset:1024\n"
"ds_read_b128 v[112:115], v0 offset:2048\n"
"ds_read_b128 v[116:119], v0 offset:3072\n"
"ds_read_b128 v[128:131], v5 offset:0\n"
"ds_read_b128 v[132:135], v5 offset:16\n"
"ds_read_b128 v[136:139], v5 offset:512\n"
"ds_read_b128 v[140:143], v5 offset:528\n"
"s_waitcnt lgkmcnt(7)\n"
"v_mfma_f32_16x16x32_bf16 v[88:91], a[0:3], v[104:107], 0\n"
"ds_read_b128 v[120:123], v0 offset:4096\n"
"v_mfma_f32_16x16x32_bf16 v[92:95], a[80:83], v[104:107], 0\n"
"ds_read_b128 v[124:127], v0 offset:5120\n"
"s_waitcnt lgkmcnt(8)\n"
"v_mfma_f32_16x16x32_bf16 v[96:99], a[0:3], v[108:111], 0\n"
"v_mfma_f32_16x16x32_bf16 v[100:103], a[80:83], v[108:111], 0\n"
"s_waitcnt lgkmcnt(7)\n"
"v_mfma_f32_16x16x32_bf16 v[88:91], a[4:7], v[112:115], v[88:91]\n"
"ds_read_b128 v[104:107], v0 offset:6144\n"
"v_mfma_f32_16x16x32_bf16 v[92:95], a[84:87], v[112:115], v[92:95]\n"
"ds_read_b128 v[108:111], v0 offset:7168\n"
"s_waitcnt lgkmcnt(8)\n"
"v_mfma_f32_16x16x32_bf16 v[96:99], a[4:7], v[116:119], v[96:99]\n"
"v_mfma_f32_16x16x32_bf16 v[100:103], a[84:87], v[116:119], v[100:103]\n"
"s_waitcnt lgkmcnt(3)\n"
"v_mfma_f32_16x16x32_bf16 v[88:91], a[8:11], v[120:123], v[88:91]\n"
"ds_read_b128 v[112:115], v0 offset:8192\n"
"v_mfma_f32_16x16x32_bf16 v[92:95], a[88:91], v[120:123], v[92:95]\n"
"ds_read_b128 v[116:119], v0 offset:9216\n"
"s_waitcnt lgkmcnt(4)\n"
"v_mfma_f32_16x16x32_bf16 v[96:99], a[8:11], v[124:127], v[96:99]\n"
"v_mfma_f32_16x16x32_bf16 v[100:103], a[88:91], v[124:127], v[100:103]\n"
"s_waitcnt lgkmcnt(3)\n"
"v_mfma_f32_16x16x32_bf16 v[88:91], a[12:15], v[104:107], v[88:91]\n"
"ds_read_b128 v[120:123], v0 offset:10240\n"
"v_mfma_f32_16x16x32_bf16 v[92:95], a[92:95], v[104:107], v[92:95]\n"
"ds_read_b128 v[124:127], v0 offset:11264\n"
"s_waitcnt lgkmcnt(4)\n"
"v_mfma_f32_16x16x32_bf16 v[96:99], a[12:15], v[108:111], v[96:99]\n"
"v_mfma_f32_16x16x32_bf16 v[100:103], a[92:95], v[108:111], v[100:103]\n"
"s_waitcnt lgkmcnt(3)\n"
"v_mfma_f32_16x16x32_bf16 v[88:91], a[16:19], v[112:115], v[88:91]\n"
"ds_read_b128 v[104:107], v0 offset:12288\n"
"v_mfma_f32_16x16x32_bf16 v[92:95], a[96:99], v[112:115], v[92:95]\n"
"ds_read_b128 v[108:111], v0 offset:13312\n"
"s_waitcnt lgkmcnt(4)\n"
"v_mfma_f32_16x16x32_bf16 v[96:99], a[16:19], v[116:119], v[96:99]\n"
"v_mfma_f32_16x16x32_bf16 v[100:103], a[96:99], v[116:119], v[100:103]\n"
"s_waitcnt lgkmcnt(3)\n"
"v_mfma_f32_16x16x32_bf16 v[88:91], a[20:23], v[120:123], v[88:91]\n"
"ds_read_b128 v[112:115], v0 offset:14336\n"
"v_pk_add_f32 v[128:129], v[128:129], v[130:131]\n"
"v_mfma_f32_16x16x32_bf16 v[92:95], a[100:103], v[120:123], v[92:95]\n"
"ds_read_b128 v[116:119], v0 offset:15360\n"
"v_pk_add_f32 v[132:133], v[132:133], v[134:135]\n"
"s_waitcnt lgkmcnt(4)\n"
"v_mfma_f32_16x16x32_bf16 v[96:99], a[20:23], v[124:127], v[96:99]\n"
"v_pk_add_f32 v[128:129], v[128:129], v[132:133]\n"
"v_mul_f32 v128, 0xbacccccd, v128\n"
"v_mfma_f32_16x16x32_bf16 v[100:103], a[100:103], v[124:127], v[100:103]\n"
"v_mul_f32 v129, 0x3acccccd, v129\n"
"v_fma_f32 v129, -v128, v128, v129\n"
"s_waitcnt lgkmcnt(3)\n"
"v_mfma_f32_16x16x32_bf16 v[88:91], a[24:27], v[104:107], v[88:91]\n"
"ds_read_b128 v[120:123], v0 offset:16384\n"
"v_max_f32 v129, 0, v129\n"
"v_mfma_f32_16x16x32_bf16 v[92:95], a[104:107], v[104:107], v[92:95]\n"
"ds_read_b128 v[124:127], v0 offset:17408\n"
"v_add_f32 v129, 0x3727c5ac, v129\n"
"s_waitcnt lgkmcnt(4)\n"
"v_mfma_f32_16x16x32_bf16 v[96:99], a[24:27], v[108:111], v[96:99]\n"
"v_rsq_f32 v130, v129\n"
"v_pk_add_f32 v[136:137], v[136:137], v[138:139]\n"
"v_mfma_f32_16x16x32_bf16 v[100:103], a[104:107], v[108:111], v[100:103]\n"
"v_pk_add_f32 v[140:141], v[140:141], v[142:143]\n"
"v_pk_add_f32 v[136:137], v[136:137], v[140:141]\n"
"s_waitcnt lgkmcnt(3)\n"
"v_mfma_f32_16x16x32_bf16 v[88:91], a[28:31], v[112:115], v[88:91]\n"
"ds_read_b128 v[104:107], v0 offset:18432\n"
"v_mul_f32 v136, 0xbacccccd, v136\n"
"v_mfma_f32_16x16x32_bf16 v[92:95], a[108:111], v[112:115], v[92:95]\n"
"ds_read_b128 v[108:111], v0 offset:19456\n"
"v_mul_f32 v137, 0x3acccccd, v137\n"
"s_waitcnt lgkmcnt(4)\n"
"v_mfma_f32_16x16x32_bf16 v[96:99], a[28:31], v[116:119], v[96:99]\n"
"v_fma_f32 v137, -v136, v136, v137\n"
"v_max_f32 v137, 0, v137\n"
"v_mfma_f32_16x16x32_bf16 v[100:103], a[108:111], v[116:119], v[100:103]\n"
"v_add_f32 v137, 0x3727c5ac, v137\n"
"v_rsq_f32 v138, v137\n"
"s_waitcnt lgkmcnt(3)\n"
"v_mfma_f32_16x16x32_bf16 v[88:91], a[32:35], v[120:123], v[88:91]\n"
"ds_read_b128 v[112:115], v0 offset:20480\n"
"v_mfma_f32_16x16x32_bf16 v[92:95], a[112:115], v[120:123], v[92:95]\n"
"ds_read_b128 v[116:119], v0 offset:21504\n"
"s_waitcnt lgkmcnt(4)\n"
"v_mfma_f32_16x16x32_bf16 v[96:99], a[32:35], v[124:127], v[96:99]\n"
"v_mfma_f32_16x16x32_bf16 v[100:103], a[112:115], v[124:127], v[100:103]\n"
"s_waitcnt lgkmcnt(3)\n"
"v_mfma_f32_16x16x32_bf16 v[88:91], a[36:39], v[104:107], v[88:91]\n"
"ds_read_b128 v[120:123], v0 offset:22528\n"
"v_mfma_f32_16x16x32_bf16 v[92:95], a[116:119], v[104:107], v[92:95]\n"
"ds_read_b128 v[124:127], v0 offset:23552\n"
"s_waitcnt lgkmcnt(4)\n"
"v_mfma_f32_16x16x32_bf16 v[96:99], a[36:39], v[108:111], v[96:99]\n"
"v_mfma_f32_16x16x32_bf16 v[100:103], a[116:119], v[108:111], v[100:103]\n"
"s_waitcnt lgkmcnt(3)\n"
"v_mfma_f32_16x16x32_bf16 v[88:91], a[40:43], v[112:115], v[88:91]\n"
"ds_read_b128 v[104:107], v0 offset:24576\n"
"v_mfma_f32_16x16x32_bf16 v[92:95], a[120:123], v[112:115], v[92:95]\n"
"ds_read_b128 v[108:111], v0 offset:25600\n"
"s_waitcnt lgkmcnt(4)\n"
"v_mfma_f32_16x16x32_bf16 v[96:99], a[40:43], v[116:119], v[96:99]\n"
"v_mfma_f32_16x16x32_bf16 v[100:103], a[120:123], v[116:119], v[100:103]\n"
"s_waitcnt lgkmcnt(3)\n"
"v_mfma_f32_16x16x32_bf16 v[88:91], a[44:47], v[120:123], v[88:91]\n"
"ds_read_b128 v[112:115], v0 offset:26624\n"
"v_mfma_f32_16x16x32_bf16 v[92:95], a[124:127], v[120:123], v[92:95]\n"
"ds_read_b128 v[116:119], v0 offset:27648\n"
"s_waitcnt lgkmcnt(4)\n"
"v_mfma_f32_16x16x32_bf16 v[96:99], a[44:47], v[124:127], v[96:99]\n"
"v_mfma_f32_16x16x32_bf16 v[100:103], a[124:127], v[124:127], v[100:103]\n"
"s_waitcnt lgkmcnt(3)\n"
"v_mfma_f32_16x16x32_bf16 v[88:91], a[48:51], v[104:107], v[88:91]\n"
"ds_read_b128 v[120:123], v0 offset:28672\n"
"v_mfma_f32_16x16x32_bf16 v[92:95], a[128:131], v[104:107], v[92:95]\n"
"ds_read_b128 v[124:127], v0 offset:29696\n"
"s_waitcnt lgkmcnt(4)\n"
"v_mfma_f32_16x16x32_bf16 v[96:99], a[48:51], v[108:111], v[96:99]\n"
"v_mfma_f32_16x16x32_bf16 v[100:103], a[128:131], v[108:111], v[100:103]\n"
"s_waitcnt lgkmcnt(3)\n"
"v_mfma_f32_16x16x32_bf16 v[88:91], a[52:55], v[112:115], v[88:91]\n"
"ds_read_b128 v[104:107], v0 offset:30720\n"
"v_mfma_f32_16x16x32_bf16 v[92:95], a[132:135], v[112:115], v[92:95]\n"
"ds_read_b128 v[108:111], v0 offset:31744\n"
"s_waitcnt lgkmcnt(4)\n"
"v_mfma_f32_16x16x32_bf16 v[96:99], a[52:55], v[116:119], v[96:99]\n"
"v_mfma_f32_16x16x32_bf16 v[100:103], a[132:135], v[116:119], v[100:103]\n"
"s_waitcnt lgkmcnt(3)\n"
"v_mfma_f32_16x16x32_bf16 v[88:91], a[56:59], v[120:123], v[88:91]\n"
"ds_read_b128 v[112:115], v0 offset:32768\n"
"v_mfma_f32_16x16x32_bf16 v[92:95], a[136:139], v[120:123], v[92:95]\n"
"ds_read_b128 v[116:119], v0 offset:33792\n"
"s_waitcnt lgkmcnt(4)\n"
"v_mfma_f32_16x16x32_bf16 v[96:99], a[56:59], v[124:127], v[96:99]\n"
"v_mfma_f32_16x16x32_bf16 v[100:103], a[136:139], v[124:127], v[100:103]\n"
"s_waitcnt lgkmcnt(3)\n"
"v_mfma_f32_16x16x32_bf16 v[88:91], a[60:63], v[104:107], v[88:91]\n"
"ds_read_b128 v[120:123], v0 offset:34816\n"
"v_mfma_f32_16x16x32_bf16 v[92:95], a[140:143], v[104:107], v[92:95]\n"
"ds_read_b128 v[124:127], v0 offset:35840\n"
"s_waitcnt lgkmcnt(4)\n"
"v_mfma_f32_16x16x32_bf16 v[96:99], a[60:63], v[108:111], v[96:99]\n"
"v_mfma_f32_16x16x32_bf16 v[100:103], a[140:143], v[108:111], v[100:103]\n"
"s_waitcnt lgkmcnt(3)\n"
"v_mfma_f32_16x16x32_bf16 v[88:91], a[64:67], v[112:115], v[88:91]\n"
"ds_read_b128 v[104:107], v0 offset:36864\n"
"v_mfma_f32_16x16x32_bf16 v[92:95], a[144:147], v[112:115], v[92:95]\n"
"ds_read_b128 v[108:111], v0 offset:37888\n"
"s_waitcnt lgkmcnt(4)\n"
"v_mfma_f32_16x16x32_bf16 v[96:99], a[64:67], v[116:119], v[96:99]\n"
"v_mfma_f32_16x16x32_bf16 v[100:103], a[144:147], v[116:119], v[100:103]\n"
"s_waitcnt lgkmcnt(3)\n"
"v_mfma_f32_16x16x32_bf16 v[88:91], a[68:71], v[120:123], v[88:91]\n"
"ds_read_b128 v[112:115], v0 offset:38912\n"
"v_mfma_f32_16x16x32_bf16 v[92:95], a[148:151], v[120:123], v[92:95]\n"
"ds_read_b128 v[116:119], v0 offset:39936\n"
"s_waitcnt lgkmcnt(4)\n"
"v_mfma_f32_16x16x32_bf16 v[96:99], a[68:71], v[124:127], v[96:99]\n"
"v_mfma_f32_16x16x32_bf16 v[100:103], a[148:151], v[124:127], v[100:103]\n"
"s_waitcnt lgkmcnt(3)\n"
"v_mfma_f32_16x16x32_bf16 v[88:91], a[72:75], v[104:107], v[88:91]\n"
"v_mfma_f32_16x16x32_bf16 v[92:95], a[152:155], v[104:107], v[92:95]\n"
"s_waitcnt lgkmcnt(2)\n"
"v_mfma_f32_16x16x32_bf16 v[96:99], a[72:75], v[108:111], v[96:99]\n"
"v_mfma_f32_16x16x32_bf16 v[100:103], a[152:155], v[108:111], v[100:103]\n"
"s_waitcnt lgkmcnt(1)\n"
"v_mfma_f32_16x16x32_bf16 v[88:91], a[76:79], v[112:115], v[88:91]\n"
"v_mfma_f32_16x16x32_bf16 v[92:95], a[156:159], v[112:115], v[92:95]\n"
"s_waitcnt lgkmcnt(0)\n"
"v_mfma_f32_16x16x32_bf16 v[96:99], a[76:79], v[116:119], v[96:99]\n"
"v_mfma_f32_16x16x32_bf16 v[100:103], a[156:159], v[116:119], v[100:103]\n"
"s_nop 3\n"
"v_pk_fma_f32 v[88:89], v[24:25], v[128:129], v[88:89] op_sel_hi:[1,0,1]\n"
"v_pk_fma_f32 v[90:91], v[26:27], v[128:129], v[90:91] op_sel_hi:[1,0,1]\n"
"v_pk_fma_f32 v[92:93], v[28:29], v[128:129], v[92:93] op_sel_hi:[1,0,1]\n"
"v_pk_fma_f32 v[94:95], v[30:31], v[128:129], v[94:95] op_sel_hi:[1,0,1]\n"
"v_pk_fma_f32 v[88:89], v[88:89], v[130:131], v[16:17] op_sel_hi:[1,0,1]\n"
"v_pk_fma_f32 v[90:91], v[90:91], v[130:131], v[18:19] op_sel_hi:[1,0,1]\n"
"v_pk_fma_f32 v[92:93], v[92:93], v[130:131], v[20:21] op_sel_hi:[1,0,1]\n"
"v_pk_fma_f32 v[94:95], v[94:95], v[130:131], v[22:23] op_sel_hi:[1,0,1]\n"
"v_pk_mul_f32 v[144:145], v[88:89], v[8:9] op_sel_hi:[1,0]\n"
"v_pk_mul_f32 v[146:147], v[90:91], v[8:9] op_sel_hi:[1,0]\n"
"v_pk_mul_f32 v[148:149], v[92:93], v[8:9] op_sel_hi:[1,0]\n"
"v_pk_mul_f32 v[150:151], v[94:95], v[8:9] op_sel_hi:[1,0]\n"
"v_min_f32 v144, 0x41f00000, v144\n"
"v_min_f32 v145, 0x41f00000, v145\n"
"v_min_f32 v146, 0x41f00000, v146\n"
"v_min_f32 v147, 0x41f00000, v147\n"
"v_min_f32 v148, 0x41f00000, v148\n"
"v_min_f32 v149, 0x41f00000, v149\n"
"v_min_f32 v150, 0x41f00000, v150\n"
"v_min_f32 v151, 0x41f00000, v151\n"
"v_exp_f32 v144, v144\n"
"v_exp_f32 v145, v145\n"
"v_exp_f32 v146, v146\n"
"v_exp_f32 v147, v147\n"
"v_exp_f32 v148, v148\n"
"v_exp_f32 v149, v149\n"
"v_exp_f32 v150, v150\n"
"v_exp_f32 v151, v151\n"
"v_pk_add_f32 v[152:153], v[144:145], v[168:169] op_sel_hi:[1,0]\n"
"v_pk_add_f32 v[154:155], v[146:147], v[168:169] op_sel_hi:[1,0]\n"
"v_pk_add_f32 v[156:157], v[148:149], v[168:169] op_sel_hi:[1,0]\n"
"v_pk_add_f32 v[158:159], v[150:151], v[168:169] op_sel_hi:[1,0]\n"
"v_pk_mul_f32 v[144:145], v[144:145], v[152:153]\n"
"v_pk_mul_f32 v[146:147], v[146:147], v[154:155]\n"
"v_pk_mul_f32 v[148:149], v[148:149], v[156:157]\n"
"v_pk_mul_f32 v[150:151], v[150:151], v[158:159]\n"
"v_pk_add_f32 v[152:153], v[144:145], v[168:169] op_sel_hi:[1,0]\n"
"v_pk_add_f32 v[154:155], v[146:147], v[168:169] op_sel_hi:[1,0]\n"
"v_pk_add_f32 v[156:157], v[148:149], v[168:169] op_sel_hi:[1,0]\n"
"v_pk_add_f32 v[158:159], v[150:151], v[168:169] op_sel_hi:[1,0]\n"
"v_rcp_f32 v152, v152\n"
"v_rcp_f32 v153, v153\n"
"v_rcp_f32 v154, v154\n"
"v_rcp_f32 v155, v155\n"
"v_rcp_f32 v156, v156\n"
"v_rcp_f32 v157, v157\n"
"v_rcp_f32 v158, v158\n"
"v_rcp_f32 v159, v159\n"
"v_pk_mul_f32 v[88:89], v[88:89], v[144:145]\n"
"v_pk_mul_f32 v[90:91], v[90:91], v[146:147]\n"
"v_pk_mul_f32 v[92:93], v[92:93], v[148:149]\n"
"v_pk_mul_f32 v[94:95], v[94:95], v[150:151]\n"
"v_pk_mul_f32 v[88:89], v[88:89], v[152:153]\n"
"v_pk_mul_f32 v[90:91], v[90:91], v[154:155]\n"
"v_pk_mul_f32 v[92:93], v[92:93], v[156:157]\n"
"v_pk_mul_f32 v[94:95], v[94:95], v[158:159]\n"
"v_cvt_pk_bf16_f32 v160, v88, v89\n"
"v_cvt_pk_bf16_f32 v161, v90, v91\n"
"v_cvt_pk_bf16_f32 v162, v92, v93\n"
"v_cvt_pk_bf16_f32 v163, v94, v95\n"
"global_store_dwordx4 v0, v[160:163], s[56:57] offset:0 sc1\n"
"v_pk_fma_f32 v[96:97], v[24:25], v[136:137], v[96:97] op_sel_hi:[1,0,1]\n"
"v_pk_fma_f32 v[98:99], v[26:27], v[136:137], v[98:99] op_sel_hi:[1,0,1]\n"
"v_pk_fma_f32 v[100:101], v[28:29], v[136:137], v[100:101] op_sel_hi:[1,0,1]\n"
"v_pk_fma_f32 v[102:103], v[30:31], v[136:137], v[102:103] op_sel_hi:[1,0,1]\n"
"v_pk_fma_f32 v[96:97], v[96:97], v[138:139], v[16:17] op_sel_hi:[1,0,1]\n"
"v_pk_fma_f32 v[98:99], v[98:99], v[138:139], v[18:19] op_sel_hi:[1,0,1]\n"
"v_pk_fma_f32 v[100:101], v[100:101], v[138:139], v[20:21] op_sel_hi:[1,0,1]\n"
"v_pk_fma_f32 v[102:103], v[102:103], v[138:139], v[22:23] op_sel_hi:[1,0,1]\n"
"v_pk_mul_f32 v[144:145], v[96:97], v[8:9] op_sel_hi:[1,0]\n"
"v_pk_mul_f32 v[146:147], v[98:99], v[8:9] op_sel_hi:[1,0]\n"
"v_pk_mul_f32 v[148:149], v[100:101], v[8:9] op_sel_hi:[1,0]\n"
"v_pk_mul_f32 v[150:151], v[102:103], v[8:9] op_sel_hi:[1,0]\n"
"v_min_f32 v144, 0x41f00000, v144\n"
"v_min_f32 v145, 0x41f00000, v145\n"
"v_min_f32 v146, 0x41f00000, v146\n"
"v_min_f32 v147, 0x41f00000, v147\n"
"v_min_f32 v148, 0x41f00000, v148\n"
"v_min_f32 v149, 0x41f00000, v149\n"
"v_min_f32 v150, 0x41f00000, v150\n"
"v_min_f32 v151, 0x41f00000, v151\n"
"v_exp_f32 v144, v144\n"
"v_exp_f32 v145, v145\n"
"v_exp_f32 v146, v146\n"
"v_exp_f32 v147, v147\n"
"v_exp_f32 v148, v148\n"
"v_exp_f32 v149, v149\n"
"v_exp_f32 v150, v150\n"
"v_exp_f32 v151, v151\n"
"v_pk_add_f32 v[152:153], v[144:145], v[168:169] op_sel_hi:[1,0]\n"
"v_pk_add_f32 v[154:155], v[146:147], v[168:169] op_sel_hi:[1,0]\n"
"v_pk_add_f32 v[156:157], v[148:149], v[168:169] op_sel_hi:[1,0]\n"
"v_pk_add_f32 v[158:159], v[150:151], v[168:169] op_sel_hi:[1,0]\n"
"v_pk_mul_f32 v[144:145], v[144:145], v[152:153]\n"
"v_pk_mul_f32 v[146:147], v[146:147], v[154:155]\n"
"v_pk_mul_f32 v[148:149], v[148:149], v[156:157]\n"
"v_pk_mul_f32 v[150:151], v[150:151], v[158:159]\n"
"v_pk_add_f32 v[152:153], v[144:145], v[168:169] op_sel_hi:[1,0]\n"
"v_pk_add_f32 v[154:155], v[146:147], v[168:169] op_sel_hi:[1,0]\n"
"v_pk_add_f32 v[156:157], v[148:149], v[168:169] op_sel_hi:[1,0]\n"
"v_pk_add_f32 v[158:159], v[150:151], v[168:169] op_sel_hi:[1,0]\n"
"v_rcp_f32 v152, v152\n"
"v_rcp_f32 v153, v153\n"
"v_rcp_f32 v154, v154\n"
"v_rcp_f32 v155, v155\n"
"v_rcp_f32 v156, v156\n"
"v_rcp_f32 v157, v157\n"
"v_rcp_f32 v158, v158\n"
"v_rcp_f32 v159, v159\n"
"v_pk_mul_f32 v[96:97], v[96:97], v[144:145]\n"
"v_pk_mul_f32 v[98:99], v[98:99], v[146:147]\n"
"v_pk_mul_f32 v[100:101], v[100:101], v[148:149]\n"
"v_pk_mul_f32 v[102:103], v[102:103], v[150:151]\n"
"v_pk_mul_f32 v[96:97], v[96:97], v[152:153]\n"
"v_pk_mul_f32 v[98:99], v[98:99], v[154:155]\n"
"v_pk_mul_f32 v[100:101], v[100:101], v[156:157]\n"
"v_pk_mul_f32 v[102:103], v[102:103], v[158:159]\n"
"v_cvt_pk_bf16_f32 v164, v96, v97\n"
"v_cvt_pk_bf16_f32 v165, v98, v99\n"
"v_cvt_pk_bf16_f32 v166, v100, v101\n"
"v_cvt_pk_bf16_f32 v167, v102, v103\n"
"global_store_dwordx4 v0, v[164:167], s[56:57] offset:1024 sc1\n"
"s_waitcnt vmcnt(0)\n"
"s_mov_b64 exec, 1\n"
"s_add_u32 s27, s25, 1\n"
"v_mov_b32 v32, s27\n"
"v_mov_b32 v33, 0\n"
"global_store_dword v33, v32, s[58:59] sc1\n"
"s_mov_b64 exec, -1\n"
"s_add_u32 s34, s34, 0x14000\n"
"s_addc_u32 s35, s35, 0\n"
"s_add_u32 s56, s56, 0x30000\n"
"s_addc_u32 s57, s57, 0\n"
"s_add_u32 s25, s25, 2\n"
"s_cmp_lt_u32 s25, 64\n"
"s_cbranch_scc1 .Lf_top_1\n"
"s_branch .Lf_end\n"
".Lf_abort:\n"
"s_mov_b64 exec, -1\n"
"s_add_u32 s28, s20, 0x617000\n"
"s_addc_u32 s29, s21, 0\n"
"v_mov_b32 v10, 0\n"
"v_mov_b32 v11, s25\n"
"v_or_b32 v11, 0x10000, v11\n"
"global_store_dword v10, v11, s[28:29] sc1\n"
".Lf_end:\n"
"s_waitcnt vmcnt(0) lgkmcnt(0)\n"
"s_branch .Lrole_end\n"
".Lrole_g:\n"
"s_mov_b64 s[20:21], %2\n"
"s_and_b32 s23, %3, 0xff\n"
"s_bfe_u32 s24, %3, 0x10008\n"
"s_bfe_u32 s22, %3, 0x2000a\n"
"s_bfe_u32 s68, %3, 0x3000c\n"
"s_mov_b32 s26, 0\n"
"s_mov_b32 s25, s24\n"
"s_mov_b64 exec, -1\n"
"s_mov_b32 s40, 0xffffffff\n"
"s_mov_b32 s41, 0x0\n"
"s_mov_b32 s42, 0xff00ff00\n"
"s_mov_b32 s43, 0xff00ff00\n"
"s_mov_b32 s44, 0x55555555\n"
"s_mov_b32 s45, 0x55555555\n"
"v_mbcnt_lo_u32_b32 v10, -1, 0\n"
"v_mbcnt_hi_u32_b32 v10, -1, v10\n"
"v_lshlrev_b32 v0, 4, v10\n"
"v_mov_b32 v11, 0x4444\n"
"v_mov_b32 v12, 0xeeee\n"
"v_cndmask_b32 v1, v12, v11, s[44:45]\n"
"s_getpc_b64 s[28:29]\n"
".Lg_pc:\n"
"s_and_b32 s28, s28, 0xffffff80\n"
"v_lshlrev_b32 v200, 3, v0\n"
"s_lshl_b32 s27, s22, 13\n"
"v_add_u32 v200, s27, v200\n"
"v_min_u32 v200, .Lg_end-.Lg_pc-4, v200\n"
"global_load_dword v200, v200, s[28:29]\n"
"s_sleep 127\n"
"s_sleep 127\n"
"s_mul_i32 s27, s22, 491520\n"
"s_lshl_b32 s28, s23, 7\n"
"s_add_u32 s27, s27, s28\n"
"s_mov_b64 s[30:31], %0\n"
"s_add_u32 s30, s30, s27\n"
"s_addc_u32 s31, s31, 0\n"
"s_mul_i32 s27, s24, 98304\n"
"s_mul_i32 s28, s22, 24576\n"
"s_add_u32 s27, s27, s28\n"
"s_add_u32 s27, s27, 0x2001000\n"
"s_add_u32 s34, s20, s27\n"
"s_addc_u32 s35, s21, 0\n"
"s_mul_i32 s27, s24, 6144\n"
"s_mul_i32 s28, s22, 1536\n"
"s_add_u32 s27, s27, s28\n"
"s_add_u32 s27, s27, 0x612000\n"
"s_add_u32 s36, s20, s27\n"
"s_addc_u32 s37, s21, 0\n"
"s_lshl_b32 s27, s23, 1\n"
"s_and_b32 s28, s22, 1\n"
"s_add_u32 s70, s27, s28\n"
"s_lshl_b32 s27, s70, 11\n"
"s_mul_i32 s28, s24, 40960\n"
"s_add_u32 s27, s27, s28\n"
"s_lshr_b32 s28, s22, 1\n"
"s_lshl_b32 s28, s28, 10\n"
"s_add_u32 s27, s27, s28\n"
"s_add_u32 s27, s27, 0x1c00000\n"
"s_add_u32 s38, s20, s27\n"
"s_addc_u32 s39, s21, 0\n"
"s_lshl_b32 s27, s24, 1\n"
"s_lshr_b32 s28, s22, 1\n"
"s_add_u32 s27, s27, s28\n"
"s_mul_i32 s27, s27, 20480\n"
"s_lshl_b32 s28, s70, 6\n"
"s_add_u32 s27, s27, s28\n"
"s_mov_b64 s[56:57], %1\n"
"s_add_u32 s56, s56, s27\n"
"s_addc_u32 s57, s57, 0\n"
"v_min_u32 v11, 11, v10\n"
"v_lshlrev_b32 v2, 7, v11\n"
"s_lshl_b32 s27, s22, 13\n"
"v_add_u32 v3, s27, v0\n"
"s_lshl_b32 s27, s22, 11\n"
"v_add_u32 v4, s27, v0\n"
"v_and_b32 v14, 15, v10\n"
"v_lshlrev_b32 v14, 4, v14\n"
"v_bfe_u32 v15, v10, 4, 1\n"
"v_lshl_add_u32 v14, v15, 3, v14\n"
"v_lshrrev_b32 v15, 5, v10\n"
"v_lshl_add_u32 v5, v15, 8, v14\n"
"v_lshrrev_b32 v11, 2, v10\n"
"v_lshlrev_b32 v11, 7, v11\n"
"v_and_b32 v12, 3, v10\n"
"v_lshl_add_u32 v11, v12, 3, v11\n"
"s_mul_i32 s27, s22, 24576\n"
"v_add_u32 v6, s27, v11\n"
"v_lshrrev_b32 v11, 3, v10\n"
"v_mul_u32_u24 v11, 1280, v11\n"
"v_and_b32 v12, 7, v10\n"
"v_lshl_add_u32 v7, v12, 4, v11\n"
"v_and_b32 v11, 15, v10\n"
"v_mul_u32_u24 v11, 1280, v11\n"
"v_lshrrev_b32 v12, 4, v10\n"
"v_lshl_add_u32 v8, v12, 3, v11\n"
"s_mul_i32 s27, s22, 24576\n"
"s_mov_b32 m0, s27\n"
"s_add_u32 s27, s27, 1024\n"
"global_load_lds_dwordx4 v7, s[30:31]\n"
"s_add_u32 s30, s30, 0x2800\n"
"s_addc_u32 s31, s31, 0\n"
"s_mov_b32 m0, s27\n"
"s_add_u32 s27, s27, 1024\n"
"global_load_lds_dwordx4 v7, s[30:31]\n"
"s_add_u32 s30, s30, 0x2800\n"
"s_addc_u32 s31, s31, 0\n"
"s_mov_b32 m0, s27\n"
"s_add_u32 s27, s27, 1024\n"
"global_load_lds_dwordx4 v7, s[30:31]\n"
"s_add_u32 s30, s30, 0x2800\n"
"s_addc_u32 s31, s31, 0\n"
"s_mov_b32 m0, s27\n"
"s_add_u32 s27, s27, 1024\n"
"global_load_lds_dwordx4 v7, s[30:31]\n"
"s_add_u32 s30, s30, 0x2800\n"
"s_addc_u32 s31, s31, 0\n"
"s_mov_b32 m0, s27\n"
"s_add_u32 s27, s27, 1024\n"
"global_load_lds_dwordx4 v7, s[30:31]\n"
"s_add_u32 s30, s30, 0x2800\n"
"s_addc_u32 s31, s31, 0\n"
"s_mov_b32 m0, s27\n"
"s_add_u32 s27, s27, 1024\n"
"global_load_lds_dwordx4 v7, s[30:31]\n"
"s_add_u32 s30, s30, 0x2800\n"
"s_addc_u32 s31, s31, 0\n"
"s_mov_b32 m0, s27\n"
"s_add_u32 s27, s27, 1024\n"
"global_load_lds_dwordx4 v7, s[30:31]\n"
"s_add_u32 s30, s30, 0x2800\n"
"s_addc_u32 s31, s31, 0\n"
"s_mov_b32 m0, s27\n"
"s_add_u32 s27, s27, 1024\n"
"global_load_lds_dwordx4 v7, s[30:31]\n"
"s_add_u32 s30, s30, 0x2800\n"
"s_addc_u32 s31, s31, 0\n"
"s_mov_b32 m0, s27\n"
"s_add_u32 s27, s27, 1024\n"
"global_load_lds_dwordx4 v7, s[30:31]\n"
"s_add_u32 s30, s30, 0x2800\n"
"s_addc_u32 s31, s31, 0\n"
"s_mov_b32 m0, s27\n"
"s_add_u32 s27, s27, 1024\n"
"global_load_lds_dwordx4 v7, s[30:31]\n"
"s_add_u32 s30, s30, 0x2800\n"
"s_addc_u32 s31, s31, 0\n"
"s_mov_b32 m0, s27\n"
"s_add_u32 s27, s27, 1024\n"
"global_load_lds_dwordx4 v7, s[30:31]\n"
"s_add_u32 s30, s30, 0x2800\n"
"s_addc_u32 s31, s31, 0\n"
"s_mov_b32 m0, s27\n"
"s_add_u32 s27, s27, 1024\n"
"global_load_lds_dwordx4 v7, s[30:31]\n"
"s_add_u32 s30, s30, 0x2800\n"
"s_addc_u32 s31, s31, 0\n"
"s_mov_b32 m0, s27\n"
"s_add_u32 s27, s27, 1024\n"
"global_load_lds_dwordx4 v7, s[30:31]\n"
"s_add_u32 s30, s30, 0x2800\n"
"s_addc_u32 s31, s31, 0\n"
"s_mov_b32 m0, s27\n"
"s_add_u32 s27, s27, 1024\n"
"global_load_lds_dwordx4 v7, s[30:31]\n"
"s_add_u32 s30, s30, 0x2800\n"
"s_addc_u32 s31, s31, 0\n"
"s_mov_b32 m0, s27\n"
"s_add_u32 s27, s27, 1024\n"
"global_load_lds_dwordx4 v7, s[30:31]\n"
"s_add_u32 s30, s30, 0x2800\n"
"s_addc_u32 s31, s31, 0\n"
"s_mov_b32 m0, s27\n"
"s_add_u32 s27, s27, 1024\n"
"global_load_lds_dwordx4 v7, s[30:31]\n"
"s_add_u32 s30, s30, 0x2800\n"
"s_addc_u32 s31, s31, 0\n"
"s_mov_b32 m0, s27\n"
"s_add_u32 s27, s27, 1024\n"
"global_load_lds_dwordx4 v7, s[30:31]\n"
"s_add_u32 s30, s30, 0x2800\n"
"s_addc_u32 s31, s31, 0\n"
"s_mov_b32 m0, s27\n"
"s_add_u32 s27, s27, 1024\n"
"global_load_lds_dwordx4 v7, s[30:31]\n"
"s_add_u32 s30, s30, 0x2800\n"
"s_addc_u32 s31, s31, 0\n"
"s_mov_b32 m0, s27\n"
"s_add_u32 s27, s27, 1024\n"
"global_load_lds_dwordx4 v7, s[30:31]\n"
"s_add_u32 s30, s30, 0x2800\n"
"s_addc_u32 s31, s31, 0\n"
"s_mov_b32 m0, s27\n"
"s_add_u32 s27, s27, 1024\n"
"global_load_lds_dwordx4 v7, s[30:31]\n"
"s_add_u32 s30, s30, 0x2800\n"
"s_addc_u32 s31, s31, 0\n"
"s_mov_b32 m0, s27\n"
"s_add_u32 s27, s27, 1024\n"
"global_load_lds_dwordx4 v7, s[30:31]\n"
"s_add_u32 s30, s30, 0x2800\n"
"s_addc_u32 s31, s31, 0\n"
"s_mov_b32 m0, s27\n"
"s_add_u32 s27, s27, 1024\n"
"global_load_lds_dwordx4 v7, s[30:31]\n"
"s_add_u32 s30, s30, 0x2800\n"
"s_addc_u32 s31, s31, 0\n"
"s_mov_b32 m0, s27\n"
"s_add_u32 s27, s27, 1024\n"
"global_load_lds_dwordx4 v7, s[30:31]\n"
"s_add_u32 s30, s30, 0x2800\n"
"s_addc_u32 s31, s31, 0\n"
"s_mov_b32 m0, s27\n"
"s_add_u32 s27, s27, 1024\n"
"global_load_lds_dwordx4 v7, s[30:31]\n"
"s_add_u32 s30, s30, 0x2800\n"
"s_addc_u32 s31, s31, 0\n"
"s_waitcnt vmcnt(0)\n"
"ds_read_b64_tr_b16 a[0:1], v6 offset:0\n"
"ds_read_b64_tr_b16 a[4:5], v6 offset:32\n"
"ds_read_b64_tr_b16 a[8:9], v6 offset:64\n"
"ds_read_b64_tr_b16 a[12:13], v6 offset:96\n"
"ds_read_b64_tr_b16 a[2:3], v6 offset:2048\n"
"ds_read_b64_tr_b16 a[6:7], v6 offset:2080\n"
"ds_read_b64_tr_b16 a[10:11], v6 offset:2112\n"
"ds_read_b64_tr_b16 a[14:15], v6 offset:2144\n"
"ds_read_b64_tr_b16 a[16:17], v6 offset:4096\n"
"ds_read_b64_tr_b16 a[20:21], v6 offset:4128\n"
"ds_read_b64_tr_b16 a[24:25], v6 offset:4160\n"
"ds_read_b64_tr_b16 a[28:29], v6 offset:4192\n"
"ds_read_b64_tr_b16 a[18:19], v6 offset:6144\n"
"ds_read_b64_tr_b16 a[22:23], v6 offset:6176\n"
"ds_read_b64_tr_b16 a[26:27], v6 offset:6208\n"
"ds_read_b64_tr_b16 a[30:31], v6 offset:6240\n"
"ds_read_b64_tr_b16 a[32:33], v6 offset:8192\n"
"ds_read_b64_tr_b16 a[36:37], v6 offset:8224\n"
"ds_read_b64_tr_b16 a[40:41], v6 offset:8256\n"
"ds_read_b64_tr_b16 a[44:45], v6 offset:8288\n"
"ds_read_b64_tr_b16 a[34:35], v6 offset:10240\n"
"ds_read_b64_tr_b16 a[38:39], v6 offset:10272\n"
"ds_read_b64_tr_b16 a[42:43], v6 offset:10304\n"
"ds_read_b64_tr_b16 a[46:47], v6 offset:10336\n"
"ds_read_b64_tr_b16 a[48:49], v6 offset:12288\n"
"ds_read_b64_tr_b16 a[52:53], v6 offset:12320\n"
"ds_read_b64_tr_b16 a[56:57], v6 offset:12352\n"
"ds_read_b64_tr_b16 a[60:61], v6 offset:12384\n"
"ds_read_b64_tr_b16 a[50:51], v6 offset:14336\n"
"ds_read_b64_tr_b16 a[54:55], v6 offset:14368\n"
"ds_read_b64_tr_b16 a[58:59], v6 offset:14400\n"
"ds_read_b64_tr_b16 a[62:63], v6 offset:14432\n"
"ds_read_b64_tr_b16 a[64:65], v6 offset:16384\n"
"ds_read_b64_tr_b16 a[68:69], v6 offset:16416\n"
"ds_read_b64_tr_b16 a[72:73], v6 offset:16448\n"
"ds_read_b64_tr_b16 a[76:77], v6 offset:16480\n"
"ds_read_b64_tr_b16 a[66:67], v6 offset:18432\n"
"ds_read_b64_tr_b16 a[70:71], v6 offset:18464\n"
"ds_read_b64_tr_b16 a[74:75], v6 offset:18496\n"
"ds_read_b64_tr_b16 a[78:79], v6 offset:18528\n"
"ds_read_b64_tr_b16 a[80:81], v6 offset:20480\n"
"ds_read_b64_tr_b16 a[84:85], v6 offset:20512\n"
"ds_read_b64_tr_b16 a[88:89], v6 offset:20544\n"
"ds_read_b64_tr_b16 a[92:93], v6 offset:20576\n"
"ds_read_b64_tr_b16 a[82:83], v6 offset:22528\n"
"ds_read_b64_tr_b16 a[86:87], v6 offset:22560\n"
"ds_read_b64_tr_b16 a[90:91], v6 offset:22592\n"
"ds_read_b64_tr_b16 a[94:95], v6 offset:22624\n"
"s_waitcnt lgkmcnt(0)\n"
"s_mul_i32 s27, s22, 24576\n"
"s_mov_b32 m0, s27\n"
"s_add_u32 s27, s27, 1024\n"
"global_load_lds_dwordx4 v7, s[30:31]\n"
"s_add_u32 s30, s30, 0x2800\n"
"s_addc_u32 s31, s31, 0\n"
"s_mov_b32 m0, s27\n"
"s_add_u32 s27, s27, 1024\n"
"global_load_lds_dwordx4 v7, s[30:31]\n"
"s_add_u32 s30, s30, 0x2800\n"
"s_addc_u32 s31, s31, 0\n"
"s_mov_b32 m0, s27\n"
"s_add_u32 s27, s27, 1024\n"
"global_load_lds_dwordx4 v7, s[30:31]\n"
"s_add_u32 s30, s30, 0x2800\n"
"s_addc_u32 s31, s31, 0\n"
"s_mov_b32 m0, s27\n"
"s_add_u32 s27, s27, 1024\n"
"global_load_lds_dwordx4 v7, s[30:31]\n"
"s_add_u32 s30, s30, 0x2800\n"
"s_addc_u32 s31, s31, 0\n"
"s_mov_b32 m0, s27\n"
"s_add_u32 s27, s27, 1024\n"
"global_load_lds_dwordx4 v7, s[30:31]\n"
"s_add_u32 s30, s30, 0x2800\n"
"s_addc_u32 s31, s31, 0\n"
"s_mov_b32 m0, s27\n"
"s_add_u32 s27, s27, 1024\n"
"global_load_lds_dwordx4 v7, s[30:31]\n"
"s_add_u32 s30, s30, 0x2800\n"
"s_addc_u32 s31, s31, 0\n"
"s_mov_b32 m0, s27\n"
"s_add_u32 s27, s27, 1024\n"
"global_load_lds_dwordx4 v7, s[30:31]\n"
"s_add_u32 s30, s30, 0x2800\n"
"s_addc_u32 s31, s31, 0\n"
"s_mov_b32 m0, s27\n"
"s_add_u32 s27, s27, 1024\n"
"global_load_lds_dwordx4 v7, s[30:31]\n"
"s_add_u32 s30, s30, 0x2800\n"
"s_addc_u32 s31, s31, 0\n"
"s_mov_b32 m0, s27\n"
"s_add_u32 s27, s27, 1024\n"
"global_load_lds_dwordx4 v7, s[30:31]\n"
"s_add_u32 s30, s30, 0x2800\n"
"s_addc_u32 s31, s31, 0\n"
"s_mov_b32 m0, s27\n"
"s_add_u32 s27, s27, 1024\n"
"global_load_lds_dwordx4 v7, s[30:31]\n"
"s_add_u32 s30, s30, 0x2800\n"
"s_addc_u32 s31, s31, 0\n"
"s_mov_b32 m0, s27\n"
"s_add_u32 s27, s27, 1024\n"
"global_load_lds_dwordx4 v7, s[30:31]\n"
"s_add_u32 s30, s30, 0x2800\n"
"s_addc_u32 s31, s31, 0\n"
"s_mov_b32 m0, s27\n"
"s_add_u32 s27, s27, 1024\n"
"global_load_lds_dwordx4 v7, s[30:31]\n"
"s_add_u32 s30, s30, 0x2800\n"
"s_addc_u32 s31, s31, 0\n"
"s_mov_b32 m0, s27\n"
"s_add_u32 s27, s27, 1024\n"
"global_load_lds_dwordx4 v7, s[30:31]\n"
"s_add_u32 s30, s30, 0x2800\n"
"s_addc_u32 s31, s31, 0\n"
"s_mov_b32 m0, s27\n"
"s_add_u32 s27, s27, 1024\n"
"global_load_lds_dwordx4 v7, s[30:31]\n"
"s_add_u32 s30, s30, 0x2800\n"
"s_addc_u32 s31, s31, 0\n"
"s_mov_b32 m0, s27\n"
"s_add_u32 s27, s27, 1024\n"
"global_load_lds_dwordx4 v7, s[30:31]\n"
"s_add_u32 s30, s30, 0x2800\n"
"s_addc_u32 s31, s31, 0\n"
"s_mov_b32 m0, s27\n"
"s_add_u32 s27, s27, 1024\n"
"global_load_lds_dwordx4 v7, s[30:31]\n"
"s_add_u32 s30, s30, 0x2800\n"
"s_addc_u32 s31, s31, 0\n"
"s_mov_b32 m0, s27\n"
"s_add_u32 s27, s27, 1024\n"
"global_load_lds_dwordx4 v7, s[30:31]\n"
"s_add_u32 s30, s30, 0x2800\n"
"s_addc_u32 s31, s31, 0\n"
"s_mov_b32 m0, s27\n"
"s_add_u32 s27, s27, 1024\n"
"global_load_lds_dwordx4 v7, s[30:31]\n"
"s_add_u32 s30, s30, 0x2800\n"
"s_addc_u32 s31, s31, 0\n"
"s_mov_b32 m0, s27\n"
"s_add_u32 s27, s27, 1024\n"
"global_load_lds_dwordx4 v7, s[30:31]\n"
"s_add_u32 s30, s30, 0x2800\n"
"s_addc_u32 s31, s31, 0\n"
"s_mov_b32 m0, s27\n"
"s_add_u32 s27, s27, 1024\n"
"global_load_lds_dwordx4 v7, s[30:31]\n"
"s_add_u32 s30, s30, 0x2800\n"
"s_addc_u32 s31, s31, 0\n"
"s_mov_b32 m0, s27\n"
"s_add_u32 s27, s27, 1024\n"
"global_load_lds_dwordx4 v7, s[30:31]\n"
"s_add_u32 s30, s30, 0x2800\n"
"s_addc_u32 s31, s31, 0\n"
"s_mov_b32 m0, s27\n"
"s_add_u32 s27, s27, 1024\n"
"global_load_lds_dwordx4 v7, s[30:31]\n"
"s_add_u32 s30, s30, 0x2800\n"
"s_addc_u32 s31, s31, 0\n"
"s_mov_b32 m0, s27\n"
"s_add_u32 s27, s27, 1024\n"
"global_load_lds_dwordx4 v7, s[30:31]\n"
"s_add_u32 s30, s30, 0x2800\n"
"s_addc_u32 s31, s31, 0\n"
"s_mov_b32 m0, s27\n"
"s_add_u32 s27, s27, 1024\n"
"global_load_lds_dwordx4 v7, s[30:31]\n"
"s_add_u32 s30, s30, 0x2800\n"
"s_addc_u32 s31, s31, 0\n"
"s_waitcnt vmcnt(0)\n"
"ds_read_b64_tr_b16 a[96:97], v6 offset:0\n"
"ds_read_b64_tr_b16 a[100:101], v6 offset:32\n"
"ds_read_b64_tr_b16 a[104:105], v6 offset:64\n"
"ds_read_b64_tr_b16 a[108:109], v6 offset:96\n"
"ds_read_b64_tr_b16 a[98:99], v6 offset:2048\n"
"ds_read_b64_tr_b16 a[102:103], v6 offset:2080\n"
"ds_read_b64_tr_b16 a[106:107], v6 offset:2112\n"
"ds_read_b64_tr_b16 a[110:111], v6 offset:2144\n"
"ds_read_b64_tr_b16 a[112:113], v6 offset:4096\n"
"ds_read_b64_tr_b16 a[116:117], v6 offset:4128\n"
"ds_read_b64_tr_b16 a[120:121], v6 offset:4160\n"
"ds_read_b64_tr_b16 a[124:125], v6 offset:4192\n"
"ds_read_b64_tr_b16 a[114:115], v6 offset:6144\n"
"ds_read_b64_tr_b16 a[118:119], v6 offset:6176\n"
"ds_read_b64_tr_b16 a[122:123], v6 offset:6208\n"
"ds_read_b64_tr_b16 a[126:127], v6 offset:6240\n"
"ds_read_b64_tr_b16 a[128:129], v6 offset:8192\n"
"ds_read_b64_tr_b16 a[132:133], v6 offset:8224\n"
"ds_read_b64_tr_b16 a[136:137], v6 offset:8256\n"
"ds_read_b64_tr_b16 a[140:141], v6 offset:8288\n"
"ds_read_b64_tr_b16 a[130:131], v6 offset:10240\n"
"ds_read_b64_tr_b16 a[134:135], v6 offset:10272\n"
"ds_read_b64_tr_b16 a[138:139], v6 offset:10304\n"
"ds_read_b64_tr_b16 a[142:143], v6 offset:10336\n"
"ds_read_b64_tr_b16 a[144:145], v6 offset:12288\n"
"ds_read_b64_tr_b16 a[148:149], v6 offset:12320\n"
"ds_read_b64_tr_b16 a[152:153], v6 offset:12352\n"
"ds_read_b64_tr_b16 a[156:157], v6 offset:12384\n"
"ds_read_b64_tr_b16 a[146:147], v6 offset:14336\n"
"ds_read_b64_tr_b16 a[150:151], v6 offset:14368\n"
"ds_read_b64_tr_b16 a[154:155], v6 offset:14400\n"
"ds_read_b64_tr_b16 a[158:159], v6 offset:14432\n"
"ds_read_b64_tr_b16 a[160:161], v6 offset:16384\n"
"ds_read_b64_tr_b16 a[164:165], v6 offset:16416\n"
"ds_read_b64_tr_b16 a[168:169], v6 offset:16448\n"
"ds_read_b64_tr_b16 a[172:173], v6 offset:16480\n"
"ds_read_b64_tr_b16 a[162:163], v6 offset:18432\n"
"ds_read_b64_tr_b16 a[166:167], v6 offset:18464\n"
"ds_read_b64_tr_b16 a[170:171], v6 offset:18496\n"
"ds_read_b64_tr_b16 a[174:175], v6 offset:18528\n"
"ds_read_b64_tr_b16 a[176:177], v6 offset:20480\n"
"ds_read_b64_tr_b16 a[180:181], v6 offset:20512\n"
"ds_read_b64_tr_b16 a[184:185], v6 offset:20544\n"
"ds_read_b64_tr_b16 a[188:189], v6 offset:20576\n"
"ds_read_b64_tr_b16 a[178:179], v6 offset:22528\n"
"ds_read_b64_tr_b16 a[182:183], v6 offset:22560\n"
"ds_read_b64_tr_b16 a[186:187], v6 offset:22592\n"
"ds_read_b64_tr_b16 a[190:191], v6 offset:22624\n"
"s_waitcnt lgkmcnt(0)\n"
"s_barrier\n"
".Lg_top_1:\n"
"s_nop 7\n"
"s_add_u32 s70, s25, 1\n"
".Lg_fw_2:\n"
"global_load_dword v9, v2, s[36:37] sc1\n"
"s_waitcnt vmcnt(0)\n"
"v_cmp_gt_u32 vcc, s70, v9\n"
"s_cbranch_vccz .Lg_fok_3\n"
"s_sleep 10\n"
"s_add_u32 s26, s26, 1\n"
"s_cmp_lt_u32 s26, 0x80000\n"
"s_cbranch_scc1 .Lg_fw_2\n"
"s_branch .Lg_abort\n"
".Lg_fok_3:\n"
"s_add_u32 s28, s34, 0x0\n"
"s_addc_u32 s29, s35, 0\n"
"global_load_dwordx4 v[16:19], v0, s[28:29] offset:-4096 sc1\n"
"global_load_dwordx4 v[20:23], v0, s[28:29] offset:-3072 sc1\n"
"global_load_dwordx4 v[24:27], v0, s[28:29] offset:-2048 sc1\n"
"global_load_dwordx4 v[28:31], v0, s[28:29] offset:-1024 sc1\n"
"global_load_dwordx4 v[32:35], v0, s[28:29] offset:0 sc1\n"
"global_load_dwordx4 v[36:39], v0, s[28:29] offset:1024 sc1\n"
"global_load_dwordx4 v[40:43], v0, s[28:29] offset:2048 sc1\n"
"global_load_dwordx4 v[44:47], v0, s[28:29] offset:3072 sc1\n"
"s_add_u32 s28, s34, 0x2000\n"
"s_addc_u32 s29, s35, 0\n"
"global_load_dwordx4 v[48:51], v0, s[28:29] offset:-4096 sc1\n"
"global_load_dwordx4 v[52:55], v0, s[28:29] offset:-3072 sc1\n"
"global_load_dwordx4 v[56:59], v0, s[28:29] offset:-2048 sc1\n"
"global_load_dwordx4 v[60:63], v0, s[28:29] offset:-1024 sc1\n"
"global_load_dwordx4 v[64:67], v0, s[28:29] offset:0 sc1\n"
"global_load_dwordx4 v[68:71], v0, s[28:29] offset:1024 sc1\n"
"global_load_dwordx4 v[72:75], v0, s[28:29] offset:2048 sc1\n"
"global_load_dwordx4 v[76:79], v0, s[28:29] offset:3072 sc1\n"
"s_add_u32 s28, s34, 0x4000\n"
"s_addc_u32 s29, s35, 0\n"
"global_load_dwordx4 v[80:83], v0, s[28:29] offset:-4096 sc1\n"
"global_load_dwordx4 v[84:87], v0, s[28:29] offset:-3072 sc1\n"
"global_load_dwordx4 v[88:91], v0, s[28:29] offset:-2048 sc1\n"
"global_load_dwordx4 v[92:95], v0, s[28:29] offset:-1024 sc1\n"
"global_load_dwordx4 v[96:99], v0, s[28:29] offset:0 sc1\n"
"global_load_dwordx4 v[100:103], v0, s[28:29] offset:1024 sc1\n"
"global_load_dwordx4 v[104:107], v0, s[28:29] offset:2048 sc1\n"
"global_load_dwordx4 v[108:111], v0, s[28:29] offset:3072 sc1\n"
"global_load_dwordx2 v[176:177], v5, s[38:39] offset:0 sc1\n"
"global_load_dwordx2 v[178:179], v5, s[38:39] offset:512 sc1\n"
"s_waitcnt vmcnt(25)\n"
"v_mfma_f32_16x16x32_bf16 v[112:115], a[0:3], v[16:19], 0\n"
"v_mfma_f32_16x16x32_bf16 v[116:119], a[4:7], v[16:19], 0\n"
"v_mfma_f32_16x16x32_bf16 v[120:123], a[8:11], v[16:19], 0\n"
"v_mfma_f32_16x16x32_bf16 v[124:127], a[12:15], v[16:19], 0\n"
"s_waitcnt vmcnt(24)\n"
"v_mfma_f32_16x16x32_bf16 v[128:131], a[0:3], v[20:23], 0\n"
"v_mfma_f32_16x16x32_bf16 v[132:135], a[4:7], v[20:23], 0\n"
"v_mfma_f32_16x16x32_bf16 v[136:139], a[8:11], v[20:23], 0\n"
"v_mfma_f32_16x16x32_bf16 v[140:143], a[12:15], v[20:23], 0\n"
"s_waitcnt vmcnt(23)\n"
"v_mfma_f32_16x16x32_bf16 v[112:115], a[16:19], v[24:27], v[112:115]\n"
"v_mfma_f32_16x16x32_bf16 v[116:119], a[20:23], v[24:27], v[116:119]\n"
"v_mfma_f32_16x16x32_bf16 v[120:123], a[24:27], v[24:27], v[120:123]\n"
"v_mfma_f32_16x16x32_bf16 v[124:127], a[28:31], v[24:27], v[124:127]\n"
"s_waitcnt vmcnt(22)\n"
"v_mfma_f32_16x16x32_bf16 v[128:131], a[16:19], v[28:31], v[128:131]\n"
"v_mfma_f32_16x16x32_bf16 v[132:135], a[20:23], v[28:31], v[132:135]\n"
"v_mfma_f32_16x16x32_bf16 v[136:139], a[24:27], v[28:31], v[136:139]\n"
"v_mfma_f32_16x16x32_bf16 v[140:143], a[28:31], v[28:31], v[140:143]\n"
"s_waitcnt vmcnt(21)\n"
"v_mfma_f32_16x16x32_bf16 v[112:115], a[32:35], v[32:35], v[112:115]\n"
"v_mfma_f32_16x16x32_bf16 v[116:119], a[36:39], v[32:35], v[116:119]\n"
"v_mfma_f32_16x16x32_bf16 v[120:123], a[40:43], v[32:35], v[120:123]\n"
"v_mfma_f32_16x16x32_bf16 v[124:127], a[44:47], v[32:35], v[124:127]\n"
"s_waitcnt vmcnt(20)\n"
"v_mfma_f32_16x16x32_bf16 v[128:131], a[32:35], v[36:39], v[128:131]\n"
"v_mfma_f32_16x16x32_bf16 v[132:135], a[36:39], v[36:39], v[132:135]\n"
"v_mfma_f32_16x16x32_bf16 v[136:139], a[40:43], v[36:39], v[136:139]\n"
"v_mfma_f32_16x16x32_bf16 v[140:143], a[44:47], v[36:39], v[140:143]\n"
"s_waitcnt vmcnt(19)\n"
"v_mfma_f32_16x16x32_bf16 v[112:115], a[48:51], v[40:43], v[112:115]\n"
"v_mfma_f32_16x16x32_bf16 v[116:119], a[52:55], v[40:43], v[116:119]\n"
"v_mfma_f32_16x16x32_bf16 v[120:123], a[56:59], v[40:43], v[120:123]\n"
"v_mfma_f32_16x16x32_bf16 v[124:127], a[60:63], v[40:43], v[124:127]\n"
"s_waitcnt vmcnt(18)\n"
"v_mfma_f32_16x16x32_bf16 v[128:131], a[48:51], v[44:47], v[128:131]\n"
"v_mfma_f32_16x16x32_bf16 v[132:135], a[52:55], v[44:47], v[132:135]\n"
"v_mfma_f32_16x16x32_bf16 v[136:139], a[56:59], v[44:47], v[136:139]\n"
"v_mfma_f32_16x16x32_bf16 v[140:143], a[60:63], v[44:47], v[140:143]\n"
"s_waitcnt vmcnt(17)\n"
"v_mfma_f32_16x16x32_bf16 v[112:115], a[64:67], v[48:51], v[112:115]\n"
"v_mfma_f32_16x16x32_bf16 v[116:119], a[68:71], v[48:51], v[116:119]\n"
"v_mfma_f32_16x16x32_bf16 v[120:123], a[72:75], v[48:51], v[120:123]\n"
"v_mfma_f32_16x16x32_bf16 v[124:127], a[76:79], v[48:51], v[124:127]\n"
"s_waitcnt vmcnt(16)\n"
"v_mfma_f32_16x16x32_bf16 v[128:131], a[64:67], v[52:55], v[128:131]\n"
"v_mfma_f32_16x16x32_bf16 v[132:135], a[68:71], v[52:55], v[132:135]\n"
"v_mfma_f32_16x16x32_bf16 v[136:139], a[72:75], v[52:55], v[136:139]\n"
"v_mfma_f32_16x16x32_bf16 v[140:143], a[76:79], v[52:55], v[140:143]\n"
"s_waitcnt vmcnt(15)\n"
"v_mfma_f32_16x16x32_bf16 v[112:115], a[80:83], v[56:59], v[112:115]\n"
"v_mfma_f32_16x16x32_bf16 v[116:119], a[84:87], v[56:59], v[116:119]\n"
"v_mfma_f32_16x16x32_bf16 v[120:123], a[88:91], v[56:59], v[120:123]\n"
"v_mfma_f32_16x16x32_bf16 v[124:127], a[92:95], v[56:59], v[124:127]\n"
"s_waitcnt vmcnt(14)\n"
"v_mfma_f32_16x16x32_bf16 v[128:131], a[80:83], v[60:63], v[128:131]\n"
"v_mfma_f32_16x16x32_bf16 v[132:135], a[84:87], v[60:63], v[132:135]\n"
"v_mfma_f32_16x16x32_bf16 v[136:139], a[88:91], v[60:63], v[136:139]\n"
"v_mfma_f32_16x16x32_bf16 v[140:143], a[92:95], v[60:63], v[140:143]\n"
"s_waitcnt vmcnt(13)\n"
"v_mfma_f32_16x16x32_bf16 v[112:115], a[96:99], v[64:67], v[112:115]\n"
"v_mfma_f32_16x16x32_bf16 v[116:119], a[100:103], v[64:67], v[116:119]\n"
"v_mfma_f32_16x16x32_bf16 v[120:123], a[104:107], v[64:67], v[120:123]\n"
"v_mfma_f32_16x16x32_bf16 v[124:127], a[108:111], v[64:67], v[124:127]\n"
"s_waitcnt vmcnt(12)\n"
"v_mfma_f32_16x16x32_bf16 v[128:131], a[96:99], v[68:71], v[128:131]\n"
"v_mfma_f32_16x16x32_bf16 v[132:135], a[100:103], v[68:71], v[132:135]\n"
"v_mfma_f32_16x16x32_bf16 v[136:139], a[104:107], v[68:71], v[136:139]\n"
"v_mfma_f32_16x16x32_bf16 v[140:143], a[108:111], v[68:71], v[140:143]\n"
"s_waitcnt vmcnt(11)\n"
"v_mfma_f32_16x16x32_bf16 v[112:115], a[112:115], v[72:75], v[112:115]\n"
"v_mfma_f32_16x16x32_bf16 v[116:119], a[116:119], v[72:75], v[116:119]\n"
"v_mfma_f32_16x16x32_bf16 v[120:123], a[120:123], v[72:75], v[120:123]\n"
"v_mfma_f32_16x16x32_bf16 v[124:127], a[124:127], v[72:75], v[124:127]\n"
"s_waitcnt vmcnt(10)\n"
"v_mfma_f32_16x16x32_bf16 v[128:131], a[112:115], v[76:79], v[128:131]\n"
"v_mfma_f32_16x16x32_bf16 v[132:135], a[116:119], v[76:79], v[132:135]\n"
"v_mfma_f32_16x16x32_bf16 v[136:139], a[120:123], v[76:79], v[136:139]\n"
"v_mfma_f32_16x16x32_bf16 v[140:143], a[124:127], v[76:79], v[140:143]\n"
"s_waitcnt vmcnt(9)\n"
"v_mfma_f32_16x16x32_bf16 v[112:115], a[128:131], v[80:83], v[112:115]\n"
"v_mfma_f32_16x16x32_bf16 v[116:119], a[132:135], v[80:83], v[116:119]\n"
"v_mfma_f32_16x16x32_bf16 v[120:123], a[136:139], v[80:83], v[120:123]\n"
"v_mfma_f32_16x16x32_bf16 v[124:127], a[140:143], v[80:83], v[124:127]\n"
"s_waitcnt vmcnt(8)\n"
"v_mfma_f32_16x16x32_bf16 v[128:131], a[128:131], v[84:87], v[128:131]\n"
"v_mfma_f32_16x16x32_bf16 v[132:135], a[132:135], v[84:87], v[132:135]\n"
"v_mfma_f32_16x16x32_bf16 v[136:139], a[136:139], v[84:87], v[136:139]\n"
"v_mfma_f32_16x16x32_bf16 v[140:143], a[140:143], v[84:87], v[140:143]\n"
"s_waitcnt vmcnt(7)\n"
"v_mfma_f32_16x16x32_bf16 v[112:115], a[144:147], v[88:91], v[112:115]\n"
"v_mfma_f32_16x16x32_bf16 v[116:119], a[148:151], v[88:91], v[116:119]\n"
"v_mfma_f32_16x16x32_bf16 v[120:123], a[152:155], v[88:91], v[120:123]\n"
"v_mfma_f32_16x16x32_bf16 v[124:127], a[156:159], v[88:91], v[124:127]\n"
"s_waitcnt vmcnt(6)\n"
"v_mfma_f32_16x16x32_bf16 v[128:131], a[144:147], v[92:95], v[128:131]\n"
"v_mfma_f32_16x16x32_bf16 v[132:135], a[148:151], v[92:95], v[132:135]\n"
"v_mfma_f32_16x16x32_bf16 v[136:139], a[152:155], v[92:95], v[136:139]\n"
"v_mfma_f32_16x16x32_bf16 v[140:143], a[156:159], v[92:95], v[140:143]\n"
"s_waitcnt vmcnt(5)\n"
"v_mfma_f32_16x16x32_bf16 v[112:115], a[160:163], v[96:99], v[112:115]\n"
"v_mfma_f32_16x16x32_bf16 v[116:119], a[164:167], v[96:99], v[116:119]\n"
"v_mfma_f32_16x16x32_bf16 v[120:123], a[168:171], v[96:99], v[120:123]\n"
"v_mfma_f32_16x16x32_bf16 v[124:127], a[172:175], v[96:99], v[124:127]\n"
"s_waitcnt vmcnt(4)\n"
"v_mfma_f32_16x16x32_bf16 v[128:131], a[160:163], v[100:103], v[128:131]\n"
"v_mfma_f32_16x16x32_bf16 v[132:135], a[164:167], v[100:103], v[132:135]\n"
"v_mfma_f32_16x16x32_bf16 v[136:139], a[168:171], v[100:103], v[136:139]\n"
"v_mfma_f32_16x16x32_bf16 v[140:143], a[172:175], v[100:103], v[140:143]\n"
"s_waitcnt vmcnt(3)\n"
"v_mfma_f32_16x16x32_bf16 v[112:115], a[176:179], v[104:107], v[112:115]\n"
"v_mfma_f32_16x16x32_bf16 v[116:119], a[180:183], v[104:107], v[116:119]\n"
"v_mfma_f32_16x16x32_bf16 v[120:123], a[184:187], v[104:107], v[120:123]\n"
"v_mfma_f32_16x16x32_bf16 v[124:127], a[188:191], v[104:107], v[124:127]\n"
"s_waitcnt vmcnt(2)\n"
"v_mfma_f32_16x16x32_bf16 v[128:131], a[176:179], v[108:111], v[128:131]\n"
"v_mfma_f32_16x16x32_bf16 v[132:135], a[180:183], v[108:111], v[132:135]\n"
"v_mfma_f32_16x16x32_bf16 v[136:139], a[184:187], v[108:111], v[136:139]\n"
"v_mfma_f32_16x16x32_bf16 v[140:143], a[188:191], v[108:111], v[140:143]\n"
"s_barrier\n"
"ds_write_b128 v3, v[112:115] offset:0\n"
"ds_write_b128 v3, v[116:119] offset:1024\n"
"ds_write_b128 v3, v[120:123] offset:2048\n"
"ds_write_b128 v3, v[124:127] offset:3072\n"
"ds_write_b128 v3, v[128:131] offset:4096\n"
"ds_write_b128 v3, v[132:135] offset:5120\n"
"ds_write_b128 v3, v[136:139] offset:6144\n"
"ds_write_b128 v3, v[140:143] offset:7168\n"
"s_waitcnt lgkmcnt(0)\n"
"s_barrier\n"
"ds_read_b128 v[144:147], v4 offset:0\n"
"ds_read_b128 v[148:151], v4 offset:8192\n"
"ds_read_b128 v[152:155], v4 offset:16384\n"
"ds_read_b128 v[156:159], v4 offset:24576\n"
"ds_read_b128 v[160:163], v4 offset:1024\n"
"ds_read_b128 v[164:167], v4 offset:9216\n"
"ds_read_b128 v[168:171], v4 offset:17408\n"
"ds_read_b128 v[172:175], v4 offset:25600\n"
"s_waitcnt vmcnt(1)\n"
"v_lshlrev_b32 v112, 16, v176\n"
"v_and_b32 v113, 0xffff0000, v176\n"
"v_lshlrev_b32 v114, 16, v177\n"
"v_and_b32 v115, 0xffff0000, v177\n"
"s_waitcnt lgkmcnt(7)\n"
"v_pk_add_f32 v[112:113], v[112:113], v[144:145]\n"
"v_pk_add_f32 v[114:115], v[114:115], v[146:147]\n"
"s_waitcnt lgkmcnt(6)\n"
"v_pk_add_f32 v[112:113], v[112:113], v[148:149]\n"
"v_pk_add_f32 v[114:115], v[114:115], v[150:151]\n"
"s_waitcnt lgkmcnt(5)\n"
"v_pk_add_f32 v[112:113], v[112:113], v[152:153]\n"
"v_pk_add_f32 v[114:115], v[114:115], v[154:155]\n"
"s_waitcnt lgkmcnt(4)\n"
"v_pk_add_f32 v[112:113], v[112:113], v[156:157]\n"
"v_pk_add_f32 v[114:115], v[114:115], v[158:159]\n"
"v_cvt_pk_bf16_f32 v112, v112, v113\n"
"v_cvt_pk_bf16_f32 v113, v114, v115\n"
"global_store_dwordx2 v8, v[112:113], s[56:57] offset:0\n"
"s_waitcnt vmcnt(1)\n"
"v_lshlrev_b32 v116, 16, v178\n"
"v_and_b32 v117, 0xffff0000, v178\n"
"v_lshlrev_b32 v118, 16, v179\n"
"v_and_b32 v119, 0xffff0000, v179\n"
"s_waitcnt lgkmcnt(3)\n"
"v_pk_add_f32 v[116:117], v[116:117], v[160:161]\n"
"v_pk_add_f32 v[118:119], v[118:119], v[162:163]\n"
"s_waitcnt lgkmcnt(2)\n"
"v_pk_add_f32 v[116:117], v[116:117], v[164:165]\n"
"v_pk_add_f32 v[118:119], v[118:119], v[166:167]\n"
"s_waitcnt lgkmcnt(1)\n"
"v_pk_add_f32 v[116:117], v[116:117], v[168:169]\n"
"v_pk_add_f32 v[118:119], v[118:119], v[170:171]\n"
"s_waitcnt lgkmcnt(0)\n"
"v_pk_add_f32 v[116:117], v[116:117], v[172:173]\n"
"v_pk_add_f32 v[118:119], v[118:119], v[174:175]\n"
"v_cvt_pk_bf16_f32 v116, v116, v117\n"
"v_cvt_pk_bf16_f32 v117, v118, v119\n"
"global_store_dwordx2 v8, v[116:117], s[56:57] offset:32\n"
"s_add_u32 s34, s34, 0x30000\n"
"s_addc_u32 s35, s35, 0\n"
"s_add_u32 s38, s38, 0x14000\n"
"s_addc_u32 s39, s39, 0\n"
"s_add_u32 s56, s56, 0x14000\n"
"s_addc_u32 s57, s57, 0\n"
"s_add_u32 s25, s25, 2\n"
"s_cmp_lt_u32 s25, 64\n"
"s_cbranch_scc1 .Lg_top_1\n"
"s_branch .Lg_end\n"
".Lg_abort:\n"
"s_mov_b64 exec, -1\n"
"s_add_u32 s28, s20, 0x617000\n"
"s_addc_u32 s29, s21, 0\n"
"v_mov_b32 v10, 0\n"
"v_mov_b32 v11, s25\n"
"v_or_b32 v11, 0x10000, v11\n"
"global_store_dword v10, v11, s[28:29] sc1\n"
".Lg_end:\n"
"s_waitcnt vmcnt(0) lgkmcnt(0)\n"
"s_branch .Lrole_end\n"
".Lrole_end:\n"
        :: "s"(p0), "s"(p1), "s"(wb), "s"(pk) : ROLE_CLOBBERS);
}

extern "C" void kernel_launch(void* const* d_in, const int* in_sizes, int n_in, void* d_out, int out_size, void* d_ws, size_t ws_size,
                              hipStream_t stream) {
    const u16* x = (const u16*)d_in[0];
    const u16* mem0 = (const u16*)d_in[1];
    const u16* Wq = (const u16*)d_in[2];
    const u16* Wk = (const u16*)d_in[3];
    const u16* Wv = (const u16*)d_in[4];
    const u16* Wo = (const u16*)d_in[5];
    const u16* w1 = (const u16*)d_in[8];
    const u16* b1 = (const u16*)d_in[9];
    const u16* w2 = (const u16*)d_in[10];
    char* wb = (char*)(((uintptr_t)d_ws + (2u << 20) - 1) & ~(uintptr_t)((2u << 20) - 1));
    constexpr int LDS = 163840;
    (void)hipFuncSetAttribute((const void*)chain_kernel, hipFuncAttributeMaxDynamicSharedMemorySize, LDS);
    chain_kernel<<<256, 256, LDS, stream>>>(Wq, Wk, Wv, Wo, x, mem0, w1, b1, w2, (u16*)d_out, wb);
}
```
